```python
import torch

def _softmax(x, axis=-1):
    return torch.softmax(x, dim=axis)
def _gelu(x):
    return torch.nn.functional.gelu(x, approximate="tanh")
def _silu(x):
    return torch.nn.functional.silu(x)
def _sigmoid(x):
    return torch.sigmoid(x)
def _layernorm(x, axis=-1, eps=1e-5):
    m = x.mean(dim=axis, keepdim=True)
    v = x.var(dim=axis, keepdim=True, unbiased=False)
    return (x - m) / torch.sqrt(v + eps)
def _scatter_add(tgt, idx, src, axis=0):
    return tgt.clone().index_add_(axis, idx, src)
def _segment_sum(src, seg, n):
    out = torch.zeros((n,) + src.shape[1:], dtype=src.dtype)
    return out.index_add_(0, seg, src)
def _pad(x, pw):
    flat = tuple(v for pair in reversed(pw) for v in pair)
    return torch.nn.functional.pad(x, flat)

kernel_name = 'gated_mix__gqa_block_cached__glu_gated_glu__decode'

def setup_inputs():

    _w = (lambda a: (lambda t: t.to(torch.bfloat16) if t.is_floating_point() else t)(torch.as_tensor(a)))
    def _A_setup():
        B, Sq, D = 8, 1, 512
        Hq, Hkv, Sk, Dh = 32, 8, 8192, 128
        assert Hq % Hkv == 0, (Hq, Hkv)
        return {
            "x": _w((lambda *s: torch.randn(*s, dtype=torch.bfloat16) * 0.05)(B, Sq, D)),
            "k_cache": _w((lambda *s: torch.randn(*s, dtype=torch.bfloat16) * 6.4)(B, Hkv, Sk, Dh)),
            "v_cache": _w((lambda *s: torch.randn(*s, dtype=torch.bfloat16) * 0.4)(B, Hkv, Sk, Dh)),
            "wq": _w((lambda *s: torch.randn(*s, dtype=torch.bfloat16) * 6.4)(D, Hq * Dh) * (D ** -0.5)),
            "wo": _w((lambda *s: torch.randn(*s, dtype=torch.bfloat16) * 0.4)(Hq * Dh, D) * ((Hq * Dh) ** -0.5)),
        }
    _A_in = _A_setup()

    def _B_setup():
        B, S, D, F = 8, 1, 512, 1792
        return {
            "x": _w((lambda *s: torch.randn(*s, dtype=torch.bfloat16) * 0.05)(B, S, D)),
            "w_gate": _w((lambda *s: torch.randn(*s, dtype=torch.bfloat16) * 0.4)(D, F)),
            "w_up": _w((lambda *s: torch.randn(*s, dtype=torch.bfloat16) * 0.05)(D, F)),
            "w_down": _w((lambda *s: torch.randn(*s, dtype=torch.bfloat16) * 0.05)(F, D)),
        }
    _B_in = _B_setup()

    return {
        "A_x": _A_in["x"],
        "A_k_cache": _A_in["k_cache"],
        "A_v_cache": _A_in["v_cache"],
        "A_wq": _A_in["wq"],
        "A_wo": _A_in["wo"],
        "B_x": _B_in["x"],
        "B_w_gate": _B_in["w_gate"],
        "B_w_up": _B_in["w_up"],
        "B_w_down": _B_in["w_down"],
        "gate_w": _w((lambda *s: torch.randn(*s, dtype=torch.bfloat16) * 0.05)(512) * 16.0),
    }

def reference(A_x, A_k_cache, A_v_cache, A_wq, A_wo, B_x, B_w_gate, B_w_up, B_w_down, gate_w):
    def _A_ref(x, k_cache, v_cache, wq, wo):
        B, Sq, D = x.shape
        Hq, Dh = 32, 128
        Hkv = k_cache.shape[1]
        rep = Hq // Hkv
        q = (lambda x, *d: x.permute(*d))((x @ wq).reshape(B, Sq, Hq, Dh), 0, 2, 1, 3)
        k = (lambda x, n, axis: torch.repeat_interleave(x, n, dim=axis))(k_cache, rep, axis=1)
        v = (lambda x, n, axis: torch.repeat_interleave(x, n, dim=axis))(v_cache, rep, axis=1)
        scores = torch.einsum("bhqd,bhkd->bhqk", q, k) * (Dh ** -0.5)
        p = _softmax(scores, axis=-1)
        o = torch.einsum("bhqk,bhkd->bhqd", p, v)
        return (lambda x, *d: x.permute(*d))(o, 0, 2, 1, 3).reshape(B, Sq, Hq * Dh) @ wo

    def _B_ref(x, w_gate, w_up, w_down):
        g = torch.einsum("bsd,df->bsf", x, w_gate)
        u = torch.einsum("bsd,df->bsf", x, w_up)
        h = _sigmoid(g) * u
        return torch.einsum("bsf,fd->bsd", h, w_down)

    x = A_x
    g = _sigmoid((x * gate_w).sum(-1, keepdims=True))
    a = _A_ref(x, A_k_cache, A_v_cache, A_wq, A_wo)
    b = _B_ref(x, B_w_gate, B_w_up, B_w_down)
    return g * a + (1 - g) * b
```

```cpp
#include <hip/hip_runtime.h>
#include <hip/hip_bf16.h>
#include <cstdint>

#define GAS __attribute__((address_space(1)))
#define LAS __attribute__((address_space(3)))
typedef unsigned v4u __attribute__((ext_vector_type(4)));
typedef unsigned v2u __attribute__((ext_vector_type(2)));
typedef float f32x4 __attribute__((ext_vector_type(4)));
typedef __bf16 bf16x8 __attribute__((ext_vector_type(8)));
typedef short s16x4 __attribute__((ext_vector_type(4)));
typedef unsigned short u16;

constexpr int NB = 8, DM = 512, HQ = 32, HKV = 8, SK = 8192, DH = 128, FF = 1792;
constexpr int NSPLIT = 4;
#define KV_AUX " nt"

__device__ __forceinline__ float bf2f(u16 v) { return __builtin_bit_cast(float, (unsigned)v << 16); }
__device__ __forceinline__ unsigned cvtpk(float lo, float hi) { unsigned r; asm("v_cvt_pk_bf16_f32 %0, %1, %2" : "=v"(r) : "v"(lo), "v"(hi)); return r; }
__device__ __forceinline__ float rbf(float x) { return __builtin_bit_cast(float, cvtpk(x, x) << 16); }
template <int CTRL> __device__ __forceinline__ float dpp(float x) {
  return __builtin_bit_cast(float, __builtin_amdgcn_mov_dpp(__builtin_bit_cast(int, x), CTRL, 0xf, 0xf, true));
}
__device__ __forceinline__ float xrow_max(float x) {
  auto s = __builtin_amdgcn_permlane16_swap(__float_as_uint(x), __float_as_uint(x), false, false);
  x = fmaxf(__uint_as_float(s[0]), __uint_as_float(s[1]));
  auto t = __builtin_amdgcn_permlane32_swap(__float_as_uint(x), __float_as_uint(x), false, false);
  return fmaxf(__uint_as_float(t[0]), __uint_as_float(t[1]));
}
__device__ __forceinline__ float xrow_sum(float x) {
  auto s = __builtin_amdgcn_permlane16_swap(__float_as_uint(x), __float_as_uint(x), false, false);
  x = __uint_as_float(s[0]) + __uint_as_float(s[1]);
  auto t = __builtin_amdgcn_permlane32_swap(__float_as_uint(x), __float_as_uint(x), false, false);
  return __uint_as_float(t[0]) + __uint_as_float(t[1]);
}
template <int N> __device__ __forceinline__ void wait_vm() { asm volatile("s_waitcnt vmcnt(%0)" ::"n"(N) : "memory"); }

template <int PAD> __device__ __forceinline__ void fill8(const void* sbase, unsigned voff, unsigned voff4, unsigned dst) {
  unsigned keep;
  asm volatile(
      "s_mov_b32 %0, m0\n\t"
      "s_mov_b32 m0, %4\n\ts_nop 0\n\t"
      "global_load_lds_dwordx4 %1, %3" KV_AUX "\n\t"
      "s_add_u32 m0, m0, %5\n\ts_nop 0\n\t"
      "global_load_lds_dwordx4 %1, %3 offset:1024" KV_AUX "\n\t"
      "s_add_u32 m0, m0, %5\n\ts_nop 0\n\t"
      "global_load_lds_dwordx4 %1, %3 offset:2048" KV_AUX "\n\t"
      "s_add_u32 m0, m0, %5\n\ts_nop 0\n\t"
      "global_load_lds_dwordx4 %1, %3 offset:3072" KV_AUX "\n\t"
      "s_add_u32 m0, m0, %6\n\ts_nop 0\n\t"
      "global_load_lds_dwordx4 %2, %3" KV_AUX "\n\t"
      "s_add_u32 m0, m0, %5\n\ts_nop 0\n\t"
      "global_load_lds_dwordx4 %2, %3 offset:1024" KV_AUX "\n\t"
      "s_add_u32 m0, m0, %5\n\ts_nop 0\n\t"
      "global_load_lds_dwordx4 %2, %3 offset:2048" KV_AUX "\n\t"
      "s_add_u32 m0, m0, %5\n\ts_nop 0\n\t"
      "global_load_lds_dwordx4 %2, %3 offset:3072" KV_AUX "\n\t"
      "s_mov_b32 m0, %0"
      : "=&s"(keep)
      : "v"(voff), "v"(voff4), "s"(sbase), "s"(dst), "n"(PAD), "n"(4096 + PAD)
      : "memory", "scc");
}

constexpr int KPAD = 16, VPAD = 32, KBLK = 1024 + KPAD, VBLK = 1024 + VPAD, KSLOT = 8 * KBLK, VSLOT = 8 * VBLK;
constexpr int RING = 2 * (KSLOT + VSLOT);
constexpr int MRG_OFF = 4 * RING;
constexpr int MRG_REC = 132;
constexpr int LDS_BYTES = MRG_OFF + 4 * 4 * MRG_REC * 4;

constexpr size_t WS_Q = 0;
constexpr size_t WS_H = 65536;
constexpr size_t WS_PML = 131072;
constexpr size_t WS_PO = 262144;

__global__ void __launch_bounds__(256) prep_kernel(const u16* __restrict__ x, const u16* __restrict__ wq, const u16* __restrict__ wg, const u16* __restrict__ wu, unsigned char* ws) {
  __shared__ float xs[NB * DM];
  __shared__ float red[4][NB][64][2];
  for (int i = threadIdx.x; i < NB * DM; i += 256) xs[i] = bf2f(x[i]);
  __syncthreads();
  const int lane = threadIdx.x & 63, w = threadIdx.x >> 6;
  const int blk = blockIdx.x;
  float acc[NB], acc2[NB];
#pragma unroll
  for (int b = 0; b < NB; ++b) acc[b] = acc2[b] = 0.f;
  if (blk < 64) {
    const int col = blk * 64 + lane;
    for (int k = w * 128; k < w * 128 + 128; ++k) {
      const float wv = bf2f(wq[(size_t)k * 4096 + col]);
#pragma unroll
      for (int b = 0; b < NB; ++b) acc[b] = fmaf(xs[b * DM + k], wv, acc[b]);
    }
  } else {
    const int col = (blk - 64) * 64 + lane;
    for (int k = w * 128; k < w * 128 + 128; ++k) {
      const float wv = bf2f(wg[(size_t)k * FF + col]), wv2 = bf2f(wu[(size_t)k * FF + col]);
#pragma unroll
      for (int b = 0; b < NB; ++b) { acc[b] = fmaf(xs[b * DM + k], wv, acc[b]); acc2[b] = fmaf(xs[b * DM + k], wv2, acc2[b]); }
    }
  }
#pragma unroll
  for (int b = 0; b < NB; ++b) { red[w][b][lane][0] = acc[b]; red[w][b][lane][1] = acc2[b]; }
  __syncthreads();
  for (int i = threadIdx.x; i < NB * 64; i += 256) {
    const int b = i >> 6, l = i & 63;
    const float s = red[0][b][l][0] + red[1][b][l][0] + red[2][b][l][0] + red[3][b][l][0];
    const float s2 = red[0][b][l][1] + red[1][b][l][1] + red[2][b][l][1] + red[3][b][l][1];
    if (blk < 64) ((u16*)(ws + WS_Q))[b * 4096 + blk * 64 + l] = (u16)(cvtpk(s, s) & 0xffff);
    else {
      const float g = rbf(s), u = rbf(s2);
      const float sg = rbf(1.f / (1.f + __expf(-g)));
      const float h = sg * u;
      ((u16*)(ws + WS_H))[b * FF + (blk - 64) * 64 + l] = (u16)(cvtpk(h, h) & 0xffff);
    }
  }
}

__global__ void __launch_bounds__(256) attn_kernel(const unsigned char* __restrict__ Kc, const unsigned char* __restrict__ Vc, unsigned char* ws) {
  extern __shared__ __attribute__((aligned(16))) unsigned char lds[];
  const int tid = threadIdx.x, lane = tid & 63, wave = __builtin_amdgcn_readfirstlane(tid >> 6);
  const int bid = blockIdx.x, kvh = bid & 7, idx = bid >> 3, b = idx >> 2, sp = idx & 3;
  const int n = lane & 15, g = lane >> 4;
  constexpr int NU = 16;
  const size_t wgbase = ((size_t)(b * HKV + kvh) * SK + (size_t)sp * 2048) * 256;
  const unsigned char* kb = Kc + wgbase + (size_t)wave * 8192;
  const unsigned char* vb = Vc + wgbase + (size_t)wave * 8192;
  const int wg = (b * HKV + kvh) * 4 + sp;
  const int rot = (wg * 5) % NU;
  const unsigned voff = (unsigned)(g * 256 + ((n ^ (g << 2)) * 16)), voff4 = voff + 4096;
  const unsigned ring = (unsigned)(size_t)(LAS unsigned char*)lds + wave * RING;
  auto uoff = [&](int u) { int t = u + rot; if (t >= NU) t -= NU; return (size_t)t * 32768; };
  bf16x8 qf[4];
  {
    const u16* q = (const u16*)(ws + WS_Q) + b * 4096 + kvh * 512 + (n & 3) * 128 + g * 32;
    asm volatile("global_load_dwordx4 %0, %4, off\n\tglobal_load_dwordx4 %1, %4, off offset:16\n\tglobal_load_dwordx4 %2, %4, off offset:32\n\tglobal_load_dwordx4 %3, %4, off offset:48"
                 : "=&v"(qf[0]), "=&v"(qf[1]), "=&v"(qf[2]), "=&v"(qf[3]) : "v"(q) : "memory");
  }
  fill8<KPAD>(kb + uoff(0), voff, voff4, ring);
  fill8<VPAD>(vb + uoff(0), voff, voff4, ring + KSLOT);
  fill8<KPAD>(kb + uoff(1), voff, voff4, ring + KSLOT + VSLOT);
  fill8<VPAD>(vb + uoff(1), voff, voff4, ring + 2 * KSLOT + VSLOT);
  asm volatile("s_waitcnt vmcnt(32)" : "+v"(qf[0]), "+v"(qf[1]), "+v"(qf[2]), "+v"(qf[3]) :: "memory");
  constexpr float C2 = 0.088388347648318440f * 1.4426950408889634f;
  const int q4 = n & 3, r4 = n >> 2;
  LAS unsigned char* lring = (LAS unsigned char*)lds + wave * RING;
  const int kbase = r4 * KBLK + q4 * 256 + ((g ^ q4) * 64);
  const int vq = r4, vp = q4;
  int vbase[4];
#pragma unroll
  for (int e2 = 0; e2 < 4; ++e2) vbase[e2] = g * VBLK + vq * 256 + ((e2 ^ vq) * 64) + (vp >> 1) * 16 + (vp & 1) * 8;
  f32x4 acc[8];
#pragma unroll
  for (int e = 0; e < 8; ++e) acc[e] = f32x4{0.f, 0.f, 0.f, 0.f};
  float m = -INFINITY, l = 0.f;
#pragma unroll 2
  for (int u = 0; u < NU; ++u) {
    const int so = (u & 1) * (KSLOT + VSLOT);
    if (u < NU - 1) wait_vm<24>(); else wait_vm<8>();
    bf16x8 kf[8];
#pragma unroll
    for (int i = 0; i < 8; ++i) kf[i] = *(const LAS bf16x8*)(lring + so + kbase + (i >> 2) * 4 * KBLK + (i & 3) * 16);
    asm volatile("s_waitcnt lgkmcnt(0)" : "+v"(kf[0]), "+v"(kf[1]), "+v"(kf[2]), "+v"(kf[3]), "+v"(kf[4]), "+v"(kf[5]), "+v"(kf[6]), "+v"(kf[7]) :: "memory");
    if (u + 2 < NU) fill8<KPAD>(kb + uoff(u + 2), voff, voff4, ring + so);
    f32x4 s0 = {0.f, 0.f, 0.f, 0.f}, s1 = {0.f, 0.f, 0.f, 0.f};
#pragma unroll
    for (int t = 0; t < 4; ++t) {
      s0 = __builtin_amdgcn_mfma_f32_16x16x32_bf16(kf[t], qf[t], s0, 0, 0, 0);
      s1 = __builtin_amdgcn_mfma_f32_16x16x32_bf16(kf[4 + t], qf[t], s1, 0, 0, 0);
    }
    float s[8];
#pragma unroll
    for (int j = 0; j < 4; ++j) { s[j] = s0[j] * C2; s[4 + j] = s1[j] * C2; }
    float pm = fmaxf(fmaxf(fmaxf(s[0], s[1]), fmaxf(s[2], s[3])), fmaxf(fmaxf(s[4], s[5]), fmaxf(s[6], s[7])));
    pm = xrow_max(pm);
    if (__any(pm > m)) {
      const float mn = fmaxf(m, pm);
      const float alpha = __builtin_amdgcn_exp2f(m - mn);
      m = mn; l *= alpha;
      const float a0 = dpp<0x00>(alpha), a1 = dpp<0x55>(alpha), a2 = dpp<0xAA>(alpha), a3 = dpp<0xFF>(alpha);
#pragma unroll
      for (int e = 0; e < 8; ++e) { acc[e][0] *= a0; acc[e][1] *= a1; acc[e][2] *= a2; acc[e][3] *= a3; }
    }
    float p[8];
#pragma unroll
    for (int j = 0; j < 8; ++j) p[j] = __builtin_amdgcn_exp2f(s[j] - m);
    l += ((p[0] + p[1]) + (p[2] + p[3])) + ((p[4] + p[5]) + (p[6] + p[7]));
    const v4u pk = {cvtpk(p[0], p[1]), cvtpk(p[2], p[3]), cvtpk(p[4], p[5]), cvtpk(p[6], p[7])};
    const bf16x8 pf = __builtin_bit_cast(bf16x8, pk);
    if (u < NU - 2) wait_vm<24>(); else if (u == NU - 2) wait_vm<16>(); else wait_vm<0>();
    s16x4 vf[16];
#pragma unroll
    for (int e = 0; e < 8; ++e) {
      vf[2 * e] = __builtin_amdgcn_ds_read_tr16_b64_v4i16((LAS s16x4*)(lring + so + KSLOT + vbase[e >> 1] + (e & 1) * 32));
      vf[2 * e + 1] = __builtin_amdgcn_ds_read_tr16_b64_v4i16((LAS s16x4*)(lring + so + KSLOT + vbase[e >> 1] + (e & 1) * 32 + 4 * VBLK));
    }
    asm volatile("s_waitcnt lgkmcnt(0)" : "+v"(vf[0]), "+v"(vf[1]), "+v"(vf[2]), "+v"(vf[3]), "+v"(vf[4]), "+v"(vf[5]), "+v"(vf[6]), "+v"(vf[7]),
                 "+v"(vf[8]), "+v"(vf[9]), "+v"(vf[10]), "+v"(vf[11]), "+v"(vf[12]), "+v"(vf[13]), "+v"(vf[14]), "+v"(vf[15]) :: "memory");
    if (u + 2 < NU) fill8<VPAD>(vb + uoff(u + 2), voff, voff4, ring + so + KSLOT);
#pragma unroll
    for (int e = 0; e < 8; ++e) {
      typedef short s16x8 __attribute__((ext_vector_type(8)));
      const s16x8 vv = __builtin_shufflevector(vf[2 * e], vf[2 * e + 1], 0, 1, 2, 3, 4, 5, 6, 7);
      acc[e] = __builtin_amdgcn_mfma_f32_16x16x32_bf16(pf, __builtin_bit_cast(bf16x8, vv), acc[e], 0, 0, 0);
    }
  }
  l = xrow_sum(l);
  LAS float* mrg = (LAS float*)((LAS unsigned char*)lds + MRG_OFF);
#pragma unroll
  for (int e = 0; e < 8; ++e) if ((e >> 1) == g) {
#pragma unroll
    for (int h = 0; h < 4; ++h) mrg[(wave * 4 + h) * MRG_REC + 4 + 16 * e + n] = acc[e][h];
  }
  if (lane < 4) { mrg[(wave * 4 + lane) * MRG_REC] = m; mrg[(wave * 4 + lane) * MRG_REC + 1] = l; }
  __syncthreads();
  {
    const int h = wave, d0 = 2 * lane;
    float M = -INFINITY;
#pragma unroll
    for (int w = 0; w < 4; ++w) M = fmaxf(M, mrg[(w * 4 + h) * MRG_REC]);
    float L = 0.f, o0 = 0.f, o1 = 0.f;
#pragma unroll
    for (int w = 0; w < 4; ++w) {
      const LAS float* rec = mrg + (w * 4 + h) * MRG_REC;
      const float wt = __builtin_amdgcn_exp2f(rec[0] - M);
      L = fmaf(wt, rec[1], L); o0 = fmaf(wt, rec[4 + d0], o0); o1 = fmaf(wt, rec[4 + d0 + 1], o1);
    }
    const size_t slot = (size_t)(b * HQ + kvh * 4 + h) * NSPLIT + sp;
    *(float2*)((float*)(ws + WS_PO) + slot * DH + d0) = make_float2(o0, o1);
    if (lane == 0) *(float2*)((float*)(ws + WS_PML) + slot * 2) = make_float2(M, L);
  }
}

__global__ void __launch_bounds__(256) post_kernel(const u16* __restrict__ x, const u16* __restrict__ wo, const u16* __restrict__ wd, const u16* __restrict__ gw, const unsigned char* ws, u16* out) {
  __shared__ float o[4096];
  __shared__ float hs[FF];
  __shared__ float red[4][64][2];
  __shared__ float gsh;
  const int b = blockIdx.x >> 3, cg = blockIdx.x & 7, tid = threadIdx.x, lane = tid & 63, w = tid >> 6;
  const float* pml = (const float*)(ws + WS_PML);
  const float* po = (const float*)(ws + WS_PO);
  for (int i = tid; i < 4096; i += 256) {
    const int h = i >> 7, d = i & 127;
    const size_t slot = (size_t)(b * HQ + h) * NSPLIT;
    float M = -INFINITY;
    for (int s = 0; s < NSPLIT; ++s) M = fmaxf(M, pml[(slot + s) * 2]);
    float L = 0.f, a = 0.f;
    for (int s = 0; s < NSPLIT; ++s) { const float wt = __builtin_amdgcn_exp2f(pml[(slot + s) * 2] - M); L = fmaf(wt, pml[(slot + s) * 2 + 1], L); a = fmaf(wt, po[(slot + s) * DH + d], a); }
    o[i] = rbf(a / L);
  }
  for (int i = tid; i < FF; i += 256) hs[i] = bf2f(((const u16*)(ws + WS_H))[b * FF + i]);
  if (w == 0) {
    float s = 0.f;
    for (int i = lane; i < DM; i += 64) s += rbf(bf2f(x[b * DM + i]) * bf2f(gw[i]));
    for (int mm = 32; mm >= 1; mm >>= 1) s += __shfl_xor(s, mm);
    s = rbf(s);
    if (lane == 0) gsh = rbf(1.f / (1.f + __expf(-s)));
  }
  __syncthreads();
  const int col = cg * 64 + lane;
  float a = 0.f, bb = 0.f;
  for (int k = w * 1024; k < w * 1024 + 1024; ++k) a = fmaf(o[k], bf2f(wo[(size_t)k * DM + col]), a);
  for (int k = w * 448; k < w * 448 + 448; ++k) bb = fmaf(hs[k], bf2f(wd[(size_t)k * DM + col]), bb);
  red[w][lane][0] = a; red[w][lane][1] = bb;
  __syncthreads();
  if (w == 0) {
    const float A = rbf(red[0][lane][0] + red[1][lane][0] + red[2][lane][0] + red[3][lane][0]);
    const float Bv = rbf(red[0][lane][1] + red[1][lane][1] + red[2][lane][1] + red[3][lane][1]);
    const float g = gsh;
    const float r = rbf(g * A) + rbf(rbf(1.f - g) * Bv);
    out[b * DM + col] = (u16)(cvtpk(r, r) & 0xffff);
  }
}

extern "C" void kernel_launch(void* const* d_in, const int* in_sizes, int n_in, void* d_out, int out_size, void* d_ws, size_t ws_size, hipStream_t stream) {
  const u16* x = (const u16*)d_in[0];
  const unsigned char* K = (const unsigned char*)d_in[1];
  const unsigned char* V = (const unsigned char*)d_in[2];
  const u16* wq = (const u16*)d_in[3];
  const u16* wo = (const u16*)d_in[4];
  const u16* wg = (const u16*)d_in[6];
  const u16* wu = (const u16*)d_in[7];
  const u16* wd = (const u16*)d_in[8];
  const u16* gw = (const u16*)d_in[9];
  unsigned char* ws = (unsigned char*)d_ws;
  (void)hipFuncSetAttribute((const void*)attn_kernel, hipFuncAttributeMaxDynamicSharedMemorySize, LDS_BYTES);
  prep_kernel<<<64 + 28, 256, 0, stream>>>(x, wq, wg, wu, ws);
  attn_kernel<<<256, 256, LDS_BYTES, stream>>>(K, V, ws);
  post_kernel<<<64, 256, 0, stream>>>(x, wo, wd, gw, ws, (u16*)d_out);
}
```

```cpp
#include <hip/hip_runtime.h>
#include <hip/hip_bf16.h>
#include <cstdint>

#define GAS __attribute__((address_space(1)))
#define LAS __attribute__((address_space(3)))
typedef unsigned v4u __attribute__((ext_vector_type(4)));
typedef unsigned v2u __attribute__((ext_vector_type(2)));
typedef float f32x4 __attribute__((ext_vector_type(4)));
typedef __bf16 bf16x8 __attribute__((ext_vector_type(8)));
typedef short s16x4 __attribute__((ext_vector_type(4)));
typedef short s16x8 __attribute__((ext_vector_type(8)));
typedef unsigned short u16;
typedef unsigned long long u64;
typedef GAS u64 gu64;
typedef GAS unsigned gu32;
#define RLX_AGENT __ATOMIC_RELAXED, __HIP_MEMORY_SCOPE_AGENT

constexpr int NB = 8, DM = 512, HQ = 32, HKV = 8, SK = 8192, DH = 128, FF = 1792;
#define KV_AUX " nt"
#define W_AUX ""

__device__ __forceinline__ float bf2f(unsigned v) { return __builtin_bit_cast(float, v << 16); }
__device__ __forceinline__ unsigned cvtpk(float lo, float hi) { unsigned r; asm("v_cvt_pk_bf16_f32 %0, %1, %2" : "=v"(r) : "v"(lo), "v"(hi)); return r; }
__device__ __forceinline__ float rbf(float x) { return __builtin_bit_cast(float, cvtpk(x, x) << 16); }
template <int CTRL> __device__ __forceinline__ float dpp(float x) {
  return __builtin_bit_cast(float, __builtin_amdgcn_mov_dpp(__builtin_bit_cast(int, x), CTRL, 0xf, 0xf, true));
}
__device__ __forceinline__ float xrow_max(float x) {
  auto s = __builtin_amdgcn_permlane16_swap(__float_as_uint(x), __float_as_uint(x), false, false);
  x = fmaxf(__uint_as_float(s[0]), __uint_as_float(s[1]));
  auto t = __builtin_amdgcn_permlane32_swap(__float_as_uint(x), __float_as_uint(x), false, false);
  return fmaxf(__uint_as_float(t[0]), __uint_as_float(t[1]));
}
__device__ __forceinline__ float xrow_sum(float x) {
  auto s = __builtin_amdgcn_permlane16_swap(__float_as_uint(x), __float_as_uint(x), false, false);
  x = __uint_as_float(s[0]) + __uint_as_float(s[1]);
  auto t = __builtin_amdgcn_permlane32_swap(__float_as_uint(x), __float_as_uint(x), false, false);
  return __uint_as_float(t[0]) + __uint_as_float(t[1]);
}
template <int N> __device__ __forceinline__ void wait_vm() { asm volatile("s_waitcnt vmcnt(%0)" ::"n"(N) : "memory"); }
__device__ __forceinline__ void wg_barrier() { asm volatile("s_waitcnt lgkmcnt(0)\n\ts_barrier" ::: "memory"); }
__device__ __forceinline__ u64 rt() { return __builtin_amdgcn_s_memrealtime(); }
__device__ __forceinline__ float sigmoidf_(float v) { return 1.f / (1.f + __expf(-v)); }

template <int PAD> __device__ __forceinline__ void fill8(const void* sbase, unsigned voff, unsigned voff4, unsigned dst) {
  unsigned keep;
  asm volatile(
      "s_mov_b32 %0, m0\n\t"
      "s_mov_b32 m0, %4\n\ts_nop 0\n\t"
      "global_load_lds_dwordx4 %1, %3" KV_AUX "\n\t"
      "s_add_u32 m0, m0, %5\n\ts_nop 0\n\t"
      "global_load_lds_dwordx4 %1, %3 offset:1024" KV_AUX "\n\t"
      "s_add_u32 m0, m0, %5\n\ts_nop 0\n\t"
      "global_load_lds_dwordx4 %1, %3 offset:2048" KV_AUX "\n\t"
      "s_add_u32 m0, m0, %5\n\ts_nop 0\n\t"
      "global_load_lds_dwordx4 %1, %3 offset:3072" KV_AUX "\n\t"
      "s_add_u32 m0, m0, %6\n\ts_nop 0\n\t"
      "global_load_lds_dwordx4 %2, %3" KV_AUX "\n\t"
      "s_add_u32 m0, m0, %5\n\ts_nop 0\n\t"
      "global_load_lds_dwordx4 %2, %3 offset:1024" KV_AUX "\n\t"
      "s_add_u32 m0, m0, %5\n\ts_nop 0\n\t"
      "global_load_lds_dwordx4 %2, %3 offset:2048" KV_AUX "\n\t"
      "s_add_u32 m0, m0, %5\n\ts_nop 0\n\t"
      "global_load_lds_dwordx4 %2, %3 offset:3072" KV_AUX "\n\t"
      "s_mov_b32 m0, %0"
      : "=&s"(keep)
      : "v"(voff), "v"(voff4), "s"(sbase), "s"(dst), "n"(PAD), "n"(4096 + PAD)
      : "memory", "scc");
}
__device__ __forceinline__ void wpiece(const void* sbase, unsigned voff, unsigned dst) {
  unsigned keep;
  asm volatile("s_mov_b32 %0, m0\n\ts_mov_b32 m0, %3\n\ts_nop 0\n\tglobal_load_lds_dwordx4 %1, %2" W_AUX "\n\ts_mov_b32 m0, %0"
               : "=&s"(keep) : "v"(voff), "s"(sbase), "s"(dst) : "memory");
}
__device__ __forceinline__ void wpiece64(const void* vaddr, unsigned dst) {
  unsigned keep;
  asm volatile("s_mov_b32 %0, m0\n\ts_mov_b32 m0, %2\n\ts_nop 0\n\tglobal_load_lds_dwordx4 %1, off" W_AUX "\n\ts_mov_b32 m0, %0"
               : "=&s"(keep) : "v"(vaddr), "s"(dst) : "memory");
}
__device__ __forceinline__ bf16x8 trfrag(LAS unsigned char* p0, LAS unsigned char* p1) {
  const s16x4 a = __builtin_amdgcn_ds_read_tr16_b64_v4i16((LAS s16x4*)p0), b = __builtin_amdgcn_ds_read_tr16_b64_v4i16((LAS s16x4*)p1);
  return __builtin_bit_cast(bf16x8, __builtin_shufflevector(a, b, 0, 1, 2, 3, 4, 5, 6, 7));
}
__device__ __forceinline__ void put(gu64* p, unsigned tag, unsigned val) { __hip_atomic_store(p, ((u64)tag << 32) | val, RLX_AGENT); }
constexpr u64 SPIN_CAP = 5000000;
template <int N> __device__ __forceinline__ void sweep(const gu64* g, int stride, unsigned tag, unsigned (&v)[N]) {
  const u64 t0 = rt();
  for (;;) {
    bool ok = true;
#pragma unroll
    for (int k = 0; k < N; ++k) { const u64 x = __hip_atomic_load(g + k * stride, RLX_AGENT); v[k] = (unsigned)x; ok &= (unsigned)(x >> 32) == tag; }
    if (__all(ok)) return;
    if (rt() - t0 > SPIN_CAP) return;
    __builtin_amdgcn_s_sleep(1);
  }
}

constexpr int KPAD = 16, VPAD = 32, KBLK = 1024 + KPAD, VBLK = 1024 + VPAD, SLOT = 8 * VBLK;
constexpr int RING = 4 * SLOT;
constexpr int MRG_OFF = 4 * RING, MRG_REC = 132;
constexpr int QP_OFF = MRG_OFF, HP_OFF = MRG_OFF + 4096;
constexpr int STG_OFF = MRG_OFF + 4 * 4 * MRG_REC * 4;
constexpr int WD_OFF = STG_OFF;
constexpr int Q_OFF = STG_OFF + 16384;
constexpr int O_OFF = Q_OFF + 1024;
constexpr int TAG_OFF = O_OFF + 1024;
constexpr int LDS_BYTES = TAG_OFF + 16;
static_assert(LDS_BYTES <= 163840, "lds");

constexpr size_t WS_E = 0;
constexpr size_t WS_QG = 4096;
constexpr size_t WS_HG = WS_QG + 8 * 8 * 256 * 8;
constexpr size_t WS_DG = WS_HG + 65536;
constexpr size_t WS_PG = WS_DG + 32 * 8 * 8 * 16 * 8;
constexpr int PG_REC = 144;
constexpr size_t WS_AG = WS_PG + (size_t)256 * 4 * PG_REC * 8;
constexpr size_t WS_ST = WS_AG + (size_t)8 * 4 * 8 * 512 * 8;

struct Args { const unsigned char *x, *K, *V, *wq, *wo, *wg, *wu, *wd, *gw; unsigned char* ws; u16* out; };

#define ST(i) do {} while (0)
#define ST_FLUSH() do {} while (0)

__global__ void __launch_bounds__(320, 1) fused_kernel(Args a) {
  extern __shared__ __attribute__((aligned(16))) unsigned char lds_[];
  LAS unsigned char* lds = (LAS unsigned char*)lds_;
  const unsigned ldsb = (unsigned)(size_t)lds;
  const int tid = threadIdx.x, lane = tid & 63, wave = __builtin_amdgcn_readfirstlane(tid >> 6);
  const int bid = blockIdx.x, xg = bid & 7, idx = bid >> 3;
  const int kvh = xg, b = idx >> 2, sp = idx & 3;
  const int n = lane & 15, g = lane >> 4, q4 = n & 3, r4 = n >> 2;

  if (wave < 4) {
    constexpr int NU = 16;
    const unsigned ring = ldsb + wave * RING;
    LAS unsigned char* lring = lds + wave * RING;
    const int tbase = (8 * g + r4) * 32 + q4 * 8;
    const size_t wgbase = ((size_t)(b * HKV + kvh) * SK + (size_t)sp * 2048) * 256;
    const unsigned char* kb = a.K + wgbase + (size_t)wave * 8192;
    const unsigned char* vb = a.V + wgbase + (size_t)wave * 8192;
    const int wg = (b * HKV + kvh) * 4 + sp;
    const int rot = (wg * 5) % NU;
    const unsigned swz = (unsigned)((n ^ (g << 2)) * 16);
    const unsigned voff = (unsigned)(g * 256) + swz, voff4 = voff + 4096;
    auto uoff = [&](int u) { int t = u + rot; if (t >= NU) t -= NU; return (size_t)t * 32768; };
    const int kbase = r4 * KBLK + q4 * 256 + ((g ^ q4) * 64);
    int vbase[4];
#pragma unroll
    for (int e2 = 0; e2 < 4; ++e2) vbase[e2] = g * VBLK + r4 * 256 + ((e2 ^ r4) * 64) + (q4 >> 1) * 16 + (q4 & 1) * 8;
    auto readK = [&](int so, bf16x8 (&kf)[8]) {
#pragma unroll
      for (int i = 0; i < 8; ++i) kf[i] = *(const LAS bf16x8*)(lring + so + kbase + (i >> 2) * 4 * KBLK + (i & 3) * 16);
      asm volatile("s_waitcnt lgkmcnt(0)" : "+v"(kf[0]), "+v"(kf[1]), "+v"(kf[2]), "+v"(kf[3]), "+v"(kf[4]), "+v"(kf[5]), "+v"(kf[6]), "+v"(kf[7]) :: "memory");
    };
    auto readV = [&](int so, bf16x8 (&vf)[8]) {
#pragma unroll
      for (int e = 0; e < 8; ++e) { LAS unsigned char* p0 = lring + so + vbase[e >> 1] + (e & 1) * 32; vf[e] = trfrag(p0, p0 + 4 * VBLK); }
      asm volatile("s_waitcnt lgkmcnt(0)" : "+v"(vf[0]), "+v"(vf[1]), "+v"(vf[2]), "+v"(vf[3]), "+v"(vf[4]), "+v"(vf[5]), "+v"(vf[6]), "+v"(vf[7]) :: "memory");
    };
    fill8<KPAD>(kb + uoff(0), voff, voff4, ring);
    bf16x8 xf[4];
    const unsigned stg = ldsb + STG_OFF + wave * 4096;
    LAS unsigned char* lstg = lds + STG_OFF + wave * 4096;
    {
      const unsigned xo = (unsigned)((n & 7) * 1024 + wave * 256 + g * 16);
      asm volatile("global_load_dwordx4 %0, %4, %5\n\tglobal_load_dwordx4 %1, %4, %5 offset:64\n\tglobal_load_dwordx4 %2, %4, %5 offset:128\n\tglobal_load_dwordx4 %3, %4, %5 offset:192"
                   : "=&v"(xf[0]), "=&v"(xf[1]), "=&v"(xf[2]), "=&v"(xf[3]) : "v"(xo), "s"(a.x) : "memory");
      const unsigned voffw = (unsigned)((wave * 128 + (lane >> 1)) * 8192 + (lane & 1) * 16);
      const unsigned char* src = a.wq + (size_t)(kvh * 512 + idx * 16) * 2;
#pragma unroll
      for (int i = 0; i < 4; ++i) wpiece(src, voffw + i * 32 * 8192, stg + i * 1024);
    }
    fill8<VPAD>(vb + uoff(0), voff, voff4, ring + SLOT);
    asm volatile("s_waitcnt vmcnt(8)" : "+v"(xf[0]), "+v"(xf[1]), "+v"(xf[2]), "+v"(xf[3]) :: "memory");
    ST(1);
    auto proj = [&](int off) {
      f32x4 c = {0.f, 0.f, 0.f, 0.f};
      bf16x8 wf_[4];
#pragma unroll
      for (int i = 0; i < 4; ++i) wf_[i] = trfrag(lstg + i * 1024 + tbase, lstg + i * 1024 + tbase + 128);
#pragma unroll
      for (int i = 0; i < 4; ++i) c = __builtin_amdgcn_mfma_f32_16x16x32_bf16(wf_[i], xf[i], c, 0, 0, 0);
      *(LAS f32x4*)(lds + off + wave * 1024 + lane * 16) = c;
    };
    proj(QP_OFF);
    wg_barrier();
    {
      const int hpc = xg * 28 + (idx < 28 ? idx : 27);
      const unsigned char* src2 = ((lane & 1) ? a.wu : a.wg) + (size_t)(wave * 128 + (lane >> 1)) * (FF * 2) + (size_t)hpc * 16;
#pragma unroll
      for (int i = 0; i < 4; ++i) wpiece64(src2 + (size_t)i * 32 * FF * 2, stg + i * 1024);
    }
    fill8<KPAD>(kb + uoff(1), voff, voff4, ring + 2 * SLOT);
    wait_vm<8>();
    fill8<VPAD>(vb + uoff(1), voff, voff4, ring + 3 * SLOT);
    proj(HP_OFF);
    wg_barrier();
    bf16x8 k0f[8], v0f[8], k1f[8], v1f[8];
    readK(0, k0f);
    wait_vm<8>();
    fill8<KPAD>(kb + uoff(2), voff, voff4, ring);
    readV(SLOT, v0f);
    wait_vm<8>();
    fill8<VPAD>(vb + uoff(2), voff, voff4, ring + SLOT);
    readK(2 * SLOT, k1f);
    wait_vm<8>();
    fill8<KPAD>(kb + uoff(3), voff, voff4, ring + 2 * SLOT);
    readV(3 * SLOT, v1f);
    wait_vm<8>();
    fill8<VPAD>(vb + uoff(3), voff, voff4, ring + 3 * SLOT);
    ST(2);
    wg_barrier();
    bf16x8 qf[4];
#pragma unroll
    for (int t = 0; t < 4; ++t) qf[t] = *(const LAS bf16x8*)(lds + Q_OFF + q4 * 256 + g * 64 + t * 16);
    const unsigned tag = __builtin_amdgcn_readfirstlane(*(const LAS unsigned*)(lds + TAG_OFF));
    constexpr float C2 = 0.088388347648318440f * 1.4426950408889634f;
    const unsigned char* wob = a.wo + ((size_t)(kvh * 512 + wave * 128) * DM + sp * 128) * 2;
    const unsigned voffo = (unsigned)(g * 1024) + swz;
    auto fillwo = [&](int s, unsigned dst) {
#pragma unroll
      for (int i = 0; i < 8; ++i) wpiece(wob, voffo + (unsigned)(s * 32 + i * 4) * 1024, dst + i * VBLK);
    };
    f32x4 acc[8];
#pragma unroll
    for (int e = 0; e < 8; ++e) acc[e] = f32x4{0.f, 0.f, 0.f, 0.f};
    float m = -INFINITY, l = 0.f;
    auto scores = [&](const bf16x8 (&kf)[8]) -> bf16x8 {
      f32x4 s0 = {0.f, 0.f, 0.f, 0.f}, s1 = {0.f, 0.f, 0.f, 0.f};
#pragma unroll
      for (int t = 0; t < 4; ++t) {
        s0 = __builtin_amdgcn_mfma_f32_16x16x32_bf16(kf[t], qf[t], s0, 0, 0, 0);
        s1 = __builtin_amdgcn_mfma_f32_16x16x32_bf16(kf[4 + t], qf[t], s1, 0, 0, 0);
      }
      float s[8];
#pragma unroll
      for (int j = 0; j < 4; ++j) { s[j] = s0[j] * C2; s[4 + j] = s1[j] * C2; }
      float pm = fmaxf(fmaxf(fmaxf(s[0], s[1]), fmaxf(s[2], s[3])), fmaxf(fmaxf(s[4], s[5]), fmaxf(s[6], s[7])));
      pm = xrow_max(pm);
      if (__any(pm > m)) {
        const float mn = fmaxf(m, pm);
        const float alpha = __builtin_amdgcn_exp2f(m - mn);
        m = mn; l *= alpha;
        const float a0 = dpp<0x00>(alpha), a1 = dpp<0x55>(alpha), a2 = dpp<0xAA>(alpha), a3 = dpp<0xFF>(alpha);
#pragma unroll
        for (int e = 0; e < 8; ++e) { acc[e][0] *= a0; acc[e][1] *= a1; acc[e][2] *= a2; acc[e][3] *= a3; }
      }
      float p[8];
#pragma unroll
      for (int j = 0; j < 8; ++j) p[j] = __builtin_amdgcn_exp2f(s[j] - m);
      l += ((p[0] + p[1]) + (p[2] + p[3])) + ((p[4] + p[5]) + (p[6] + p[7]));
      const v4u pk = {cvtpk(p[0], p[1]), cvtpk(p[2], p[3]), cvtpk(p[4], p[5]), cvtpk(p[6], p[7])};
      return __builtin_bit_cast(bf16x8, pk);
    };
    auto pv = [&](const bf16x8 pf, const bf16x8 (&vf)[8]) {
#pragma unroll
      for (int e = 0; e < 8; ++e) acc[e] = __builtin_amdgcn_mfma_f32_16x16x32_bf16(pf, vf[e], acc[e], 0, 0, 0);
    };
    pv(scores(k0f), v0f);
    pv(scores(k1f), v1f);
#pragma unroll 2
    for (int u = 2; u < NU; ++u) {
      const int so = (u & 1) * 2 * SLOT;
      wait_vm<24>();
      bf16x8 kf[8];
      readK(so, kf);
      if (u + 2 < NU) fill8<KPAD>(kb + uoff(u + 2), voff, voff4, ring + so); else fillwo((u & 1) * 2, ring + so);
      const bf16x8 pf = scores(kf);
      wait_vm<24>();
      bf16x8 vf[8];
      readV(so + SLOT, vf);
      if (u + 2 < NU) fill8<VPAD>(vb + uoff(u + 2), voff, voff4, ring + so + SLOT); else fillwo((u & 1) * 2 + 1, ring + so + SLOT);
      pv(pf, vf);
    }
    ST(3);
    l = xrow_sum(l);
    LAS float* mrg = (LAS float*)(lds + MRG_OFF);
#pragma unroll
    for (int e = 0; e < 8; ++e) if ((e >> 1) == g) {
#pragma unroll
      for (int h = 0; h < 4; ++h) mrg[(wave * 4 + h) * MRG_REC + 4 + 16 * e + n] = acc[e][h];
    }
    if (lane < 4) { mrg[(wave * 4 + lane) * MRG_REC] = m; mrg[(wave * 4 + lane) * MRG_REC + 1] = l; }
    wg_barrier();
    const int d0 = 2 * lane;
    float M = -INFINITY;
#pragma unroll
    for (int w = 0; w < 4; ++w) M = fmaxf(M, mrg[(w * 4 + wave) * MRG_REC]);
    float L = 0.f, o0 = 0.f, o1 = 0.f;
#pragma unroll
    for (int w = 0; w < 4; ++w) {
      const LAS float* rec = mrg + (w * 4 + wave) * MRG_REC;
      const float wt = __builtin_amdgcn_exp2f(rec[0] - M);
      L = fmaf(wt, rec[1], L); o0 = fmaf(wt, rec[4 + d0], o0); o1 = fmaf(wt, rec[4 + d0 + 1], o1);
    }
    gu64* pg = (gu64*)(a.ws + WS_PG);
    {
      gu64* mine = pg + (size_t)(wg * 4 + wave) * PG_REC;
      put(mine + lane, tag, __float_as_uint(o0));
      put(mine + 64 + lane, tag, __float_as_uint(o1));
      if (lane < 2) put(mine + 128 + lane, tag, __float_as_uint(lane ? L : M));
    }
    ST(4);
    wait_vm<3>();
    bf16x8 wf[32];
#pragma unroll
    for (int s = 0; s < 4; ++s)
#pragma unroll
      for (int e = 0; e < 8; ++e) {
        LAS unsigned char* p0 = lring + s * SLOT + vbase[e >> 1] + (e & 1) * 32;
        wf[s * 8 + e] = trfrag(p0, p0 + 4 * VBLK);
      }
    ST(5);
    float pm_[4], pl_[4], p0_[4], p1_[4];
    {
      const gu64* grp = pg + (size_t)((wg & ~3) * 4 + wave) * PG_REC;
      const u64 t0 = rt();
      for (;;) {
        bool ok = true;
#pragma unroll
        for (int s = 0; s < 4; ++s) {
          const gu64* r = grp + (size_t)s * 4 * PG_REC;
          const u64 x0 = __hip_atomic_load(r + lane, RLX_AGENT), x1 = __hip_atomic_load(r + 64 + lane, RLX_AGENT);
          const u64 xm = __hip_atomic_load(r + 128, RLX_AGENT), xl = __hip_atomic_load(r + 129, RLX_AGENT);
          ok &= (unsigned)(x0 >> 32) == tag && (unsigned)(x1 >> 32) == tag && (unsigned)(xm >> 32) == tag && (unsigned)(xl >> 32) == tag;
          p0_[s] = __uint_as_float((unsigned)x0); p1_[s] = __uint_as_float((unsigned)x1); pm_[s] = __uint_as_float((unsigned)xm); pl_[s] = __uint_as_float((unsigned)xl);
        }
        if (__all(ok)) break;
        if (rt() - t0 > SPIN_CAP) break;
      }
    }
    ST(6);
    {
      const float MM = fmaxf(fmaxf(pm_[0], pm_[1]), fmaxf(pm_[2], pm_[3]));
      float LL = 0.f, y0 = 0.f, y1 = 0.f;
#pragma unroll
      for (int s = 0; s < 4; ++s) { const float wt = __builtin_amdgcn_exp2f(pm_[s] - MM); LL = fmaf(wt, pl_[s], LL); y0 = fmaf(wt, p0_[s], y0); y1 = fmaf(wt, p1_[s], y1); }
      const float inv = 1.f / LL;
      *(LAS unsigned*)(lds + O_OFF + wave * 256 + lane * 4) = cvtpk(y0 * inv, y1 * inv);
    }
    asm volatile("s_waitcnt lgkmcnt(0)" ::: "memory");
    f32x4 ac[8];
#pragma unroll
    for (int e = 0; e < 8; ++e) ac[e] = f32x4{0.f, 0.f, 0.f, 0.f};
#pragma unroll
    for (int s = 0; s < 4; ++s) {
      const v2u lo = *(const LAS v2u*)(lds + O_OFF + wave * 256 + s * 64 + g * 8), hi = *(const LAS v2u*)(lds + O_OFF + wave * 256 + s * 64 + 32 + g * 8);
      const v4u of = {lo.x, lo.y, hi.x, hi.y};
#pragma unroll
      for (int e = 0; e < 8; ++e) ac[e] = __builtin_amdgcn_mfma_f32_16x16x32_bf16(__builtin_bit_cast(bf16x8, of), wf[s * 8 + e], ac[e], 0, 0, 0);
    }
    {
      gu64* ag = (gu64*)(a.ws + WS_AG) + (size_t)((b * 4 + sp) * 8) * 512 + (kvh * 4 + wave) * 16 + n;
      const float v0 = g == 0 ? ac[0][0] : g == 1 ? ac[2][0] : g == 2 ? ac[4][0] : ac[6][0];
      const float v1 = g == 0 ? ac[1][0] : g == 1 ? ac[3][0] : g == 2 ? ac[5][0] : ac[7][0];
      put(ag + (size_t)(2 * g) * 512, tag, __float_as_uint(v0));
      put(ag + (size_t)(2 * g + 1) * 512, tag, __float_as_uint(v1));
    }
    ST(7);
    ST_FLUSH();
  } else {
    __builtin_amdgcn_s_setprio(2);
    const unsigned tag = __builtin_amdgcn_readfirstlane(__hip_atomic_load((const gu32*)(a.ws + WS_E), RLX_AGENT)) + 1u;
    const v4u xg8 = *(const GAS v4u*)(a.x + (size_t)b * 1024 + lane * 16), gw8 = *(const GAS v4u*)(a.gw + lane * 16);
    const int tbase = (8 * g + r4) * 32 + q4 * 8;
    float gate;
    {
      float s = 0.f;
#pragma unroll
      for (int k = 0; k < 4; ++k) {
        s += rbf(bf2f(xg8[k] & 0xffffu) * bf2f(gw8[k] & 0xffffu));
        s += rbf(bf2f(xg8[k] >> 16) * bf2f(gw8[k] >> 16));
      }
      s += dpp<0xB1>(s); s += dpp<0x4E>(s); s += dpp<0x141>(s); s += dpp<0x128>(s);
      s = xrow_sum(s);
      gate = rbf(sigmoidf_(rbf(s)));
    }
    if (lane == 0) *(LAS unsigned*)(lds + TAG_OFF) = tag;
    wg_barrier();
    {
      const LAS f32x4* qp = (const LAS f32x4*)(lds + QP_OFF) + lane;
      const f32x4 c0 = (qp[0] + qp[64]) + (qp[128] + qp[192]);
      const unsigned val = n < 8 ? cvtpk(c0[0], c0[1]) : cvtpk(c0[2], c0[3]);
      put((gu64*)(a.ws + WS_QG) + (size_t)(kvh * 8 + (n & 7)) * 256 + idx * 8 + g * 2 + (n >> 3), tag, val);
    }
    ST(1);
    wg_barrier();
    if (idx < 28) {
      const LAS f32x4* hp_ = (const LAS f32x4*)(lds + HP_OFF) + lane;
      const f32x4 c0 = (hp_[0] + hp_[64]) + (hp_[128] + hp_[192]);
      float hv[4];
#pragma unroll
      for (int j = 0; j < 4; ++j) {
        auto t = __builtin_amdgcn_permlane32_swap(__float_as_uint(c0[j]), __float_as_uint(c0[j]), false, false);
        hv[j] = rbf(sigmoidf_(rbf(__uint_as_float(t[0])))) * rbf(__uint_as_float(t[1]));
      }
      const unsigned val = n < 8 ? cvtpk(hv[0], hv[1]) : cvtpk(hv[2], hv[3]);
      if (g < 2) put((gu64*)(a.ws + WS_HG) + (size_t)((xg * 28 + idx) * 8 + (n & 7)) * 4 + g * 2 + (n >> 3), tag, val);
    }
    ST(2);
    {
      unsigned qv[4];
      sweep<4>((const gu64*)(a.ws + WS_QG) + (size_t)(kvh * 8 + b) * 256 + lane, 64, tag, qv);
#pragma unroll
      for (int k = 0; k < 4; ++k) *(LAS unsigned*)(lds + Q_OFF + (lane + 64 * k) * 4) = qv[k];
    }
    {
      const unsigned voffd = (unsigned)((lane >> 1) * 1024 + (lane & 1) * 16);
      const unsigned char* src = a.wd + ((size_t)(xg * 224) * DM + idx * 16) * 2;
#pragma unroll
      for (int i = 0; i < 7; ++i) wpiece(src, voffd + i * 32 * 1024, ldsb + WD_OFF + i * 1024);
    }
    unsigned hv[14];
    const gu64* hg = (const gu64*)(a.ws + WS_HG) + (size_t)((xg * 28 + g) * 8 + (n & 7)) * 4 + (n >> 3) * 2;
    u64 hx[14];
#pragma unroll
    for (int s = 0; s < 7; ++s) { hx[2 * s] = __hip_atomic_load(hg + s * 128, RLX_AGENT); hx[2 * s + 1] = __hip_atomic_load(hg + s * 128 + 1, RLX_AGENT); }
    ST(3);
    wg_barrier();
    __builtin_amdgcn_s_setprio(0);
    {
      const u64 t0 = rt();
      for (;;) {
        bool ok = true;
#pragma unroll
        for (int k = 0; k < 14; ++k) { hv[k] = (unsigned)hx[k]; ok &= (unsigned)(hx[k] >> 32) == tag; }
        if (__all(ok)) break;
        if (rt() - t0 > SPIN_CAP) break;
        __builtin_amdgcn_s_sleep(8);
#pragma unroll
        for (int s = 0; s < 7; ++s) { hx[2 * s] = __hip_atomic_load(hg + s * 128, RLX_AGENT); hx[2 * s + 1] = __hip_atomic_load(hg + s * 128 + 1, RLX_AGENT); }
      }
      wait_vm<0>();
      f32x4 c0 = {0.f, 0.f, 0.f, 0.f};
#pragma unroll
      for (int s = 0; s < 7; ++s) {
        const unsigned o0 = (unsigned)__builtin_amdgcn_mov_dpp((int)hv[2 * s], 0x128, 0xf, 0xf, true), o1 = (unsigned)__builtin_amdgcn_mov_dpp((int)hv[2 * s + 1], 0x128, 0xf, 0xf, true);
        const v4u hf = n < 8 ? v4u{hv[2 * s], hv[2 * s + 1], o0, o1} : v4u{o0, o1, hv[2 * s], hv[2 * s + 1]};
        c0 = __builtin_amdgcn_mfma_f32_16x16x32_bf16(__builtin_bit_cast(bf16x8, hf), trfrag(lds + WD_OFF + s * 1024 + tbase, lds + WD_OFF + s * 1024 + tbase + 128), c0, 0, 0, 0);
      }
      gu64* dg = (gu64*)(a.ws + WS_DG) + (size_t)(idx * 8 + (g & 1) * 4 + (g >> 1) * 2) * 128 + xg * 16 + n;
      put(dg, tag, __float_as_uint(g < 2 ? c0[0] : c0[2]));
      put(dg + 128, tag, __float_as_uint(g < 2 ? c0[1] : c0[3]));
    }
    ST(4);
    const int c16 = sp * 8 + kvh;
    wg_barrier();
    ST(5);
    float bm;
    {
      unsigned dv[2];
      sweep<2>((const gu64*)(a.ws + WS_DG) + (size_t)(c16 * 8 + b) * 128 + lane, 64, tag, dv);
      bm = rbf(xrow_sum(__uint_as_float(dv[0]) + __uint_as_float(dv[1])));
    }
    ST(6);
    {
      unsigned av[8];
      sweep<8>((const gu64*)(a.ws + WS_AG) + (size_t)((b * 4 + sp) * 8 + kvh) * 512 + lane, 64, tag, av);
      float s = 0.f;
#pragma unroll
      for (int k = 0; k < 8; ++k) s += __uint_as_float(av[k]);
      const float A = rbf(xrow_sum(s));
      const float r = rbf(gate * A) + rbf(rbf(1.f - gate) * bm);
      if (lane < 16) a.out[b * DM + c16 * 16 + lane] = (u16)(cvtpk(r, r) & 0xffffu);
      if (bid == 0 && lane == 0) __hip_atomic_store((gu32*)(a.ws + WS_E), tag, RLX_AGENT);
    }
    ST(7);
    ST_FLUSH();
  }
}

extern "C" void kernel_launch(void* const* d_in, const int* in_sizes, int n_in, void* d_out, int out_size, void* d_ws, size_t ws_size, hipStream_t stream) {
  Args a;
  a.x = (const unsigned char*)d_in[0]; a.K = (const unsigned char*)d_in[1]; a.V = (const unsigned char*)d_in[2];
  a.wq = (const unsigned char*)d_in[3]; a.wo = (const unsigned char*)d_in[4];
  a.wg = (const unsigned char*)d_in[6]; a.wu = (const unsigned char*)d_in[7]; a.wd = (const unsigned char*)d_in[8]; a.gw = (const unsigned char*)d_in[9];
  a.ws = (unsigned char*)d_ws; a.out = (u16*)d_out;
  (void)hipFuncSetAttribute((const void*)fused_kernel, hipFuncAttributeMaxDynamicSharedMemorySize, LDS_BYTES);
  fused_kernel<<<256, 320, LDS_BYTES, stream>>>(a);
}
```

```cpp
#include <hip/hip_runtime.h>
#include <hip/hip_bf16.h>
#include <cstdint>

#define GAS __attribute__((address_space(1)))
#define LAS __attribute__((address_space(3)))
typedef unsigned v4u __attribute__((ext_vector_type(4)));
typedef unsigned v2u __attribute__((ext_vector_type(2)));
typedef float f32x4 __attribute__((ext_vector_type(4)));
typedef __bf16 bf16x8 __attribute__((ext_vector_type(8)));
typedef short s16x4 __attribute__((ext_vector_type(4)));
typedef short s16x8 __attribute__((ext_vector_type(8)));
typedef unsigned short u16;
typedef unsigned long long u64;
typedef GAS u64 gu64;
typedef GAS unsigned gu32;
#define RLX_AGENT __ATOMIC_RELAXED, __HIP_MEMORY_SCOPE_AGENT

constexpr int NB = 8, DM = 512, HQ = 32, HKV = 8, SK = 8192, DH = 128, FF = 1792;
#define W_AUX ""

__device__ __forceinline__ float bf2f(unsigned v) { return __builtin_bit_cast(float, v << 16); }
__device__ __forceinline__ unsigned cvtpk(float lo, float hi) { unsigned r; asm("v_cvt_pk_bf16_f32 %0, %1, %2" : "=v"(r) : "v"(lo), "v"(hi)); return r; }
__device__ __forceinline__ float rbf(float x) { return __builtin_bit_cast(float, cvtpk(x, x) << 16); }
template <int CTRL> __device__ __forceinline__ float dpp(float x) {
  return __builtin_bit_cast(float, __builtin_amdgcn_mov_dpp(__builtin_bit_cast(int, x), CTRL, 0xf, 0xf, true));
}
__device__ __forceinline__ float xrow_max(float x) {
  auto s = __builtin_amdgcn_permlane16_swap(__float_as_uint(x), __float_as_uint(x), false, false);
  x = fmaxf(__uint_as_float(s[0]), __uint_as_float(s[1]));
  auto t = __builtin_amdgcn_permlane32_swap(__float_as_uint(x), __float_as_uint(x), false, false);
  return fmaxf(__uint_as_float(t[0]), __uint_as_float(t[1]));
}
__device__ __forceinline__ float xrow_sum(float x) {
  auto s = __builtin_amdgcn_permlane16_swap(__float_as_uint(x), __float_as_uint(x), false, false);
  x = __uint_as_float(s[0]) + __uint_as_float(s[1]);
  auto t = __builtin_amdgcn_permlane32_swap(__float_as_uint(x), __float_as_uint(x), false, false);
  return __uint_as_float(t[0]) + __uint_as_float(t[1]);
}
template <int N> __device__ __forceinline__ void wait_vm() { asm volatile("s_waitcnt vmcnt(%0)" ::"n"(N) : "memory"); }
__device__ __forceinline__ void wg_barrier() { asm volatile("s_waitcnt lgkmcnt(0)\n\ts_barrier" ::: "memory"); }
__device__ __forceinline__ u64 rt() { return __builtin_amdgcn_s_memrealtime(); }
__device__ __forceinline__ float sigmoidf_(float v) { return 1.f / (1.f + __expf(-v)); }

#define FILL8_BODY(KV_AUX) asm volatile( \
      "s_mov_b32 %0, m0\n\t" \
      "s_mov_b32 m0, %4\n\ts_nop 0\n\t" \
      "global_load_lds_dwordx4 %1, %3" KV_AUX "\n\t" \
      "s_add_u32 m0, m0, %5\n\ts_nop 0\n\t" \
      "global_load_lds_dwordx4 %1, %3 offset:1024" KV_AUX "\n\t" \
      "s_add_u32 m0, m0, %5\n\ts_nop 0\n\t" \
      "global_load_lds_dwordx4 %1, %3 offset:2048" KV_AUX "\n\t" \
      "s_add_u32 m0, m0, %5\n\ts_nop 0\n\t" \
      "global_load_lds_dwordx4 %1, %3 offset:3072" KV_AUX "\n\t" \
      "s_add_u32 m0, m0, %6\n\ts_nop 0\n\t" \
      "global_load_lds_dwordx4 %2, %3" KV_AUX "\n\t" \
      "s_add_u32 m0, m0, %5\n\ts_nop 0\n\t" \
      "global_load_lds_dwordx4 %2, %3 offset:1024" KV_AUX "\n\t" \
      "s_add_u32 m0, m0, %5\n\ts_nop 0\n\t" \
      "global_load_lds_dwordx4 %2, %3 offset:2048" KV_AUX "\n\t" \
      "s_add_u32 m0, m0, %5\n\ts_nop 0\n\t" \
      "global_load_lds_dwordx4 %2, %3 offset:3072" KV_AUX "\n\t" \
      "s_mov_b32 m0, %0" \
      : "=&s"(keep) \
      : "v"(voff), "v"(voff4), "s"(sbase), "s"(dst), "n"(PAD), "n"(4096 + PAD) \
      : "memory", "scc")
template <int PAD, int NT> __device__ __forceinline__ void fill8p(const void* sbase, unsigned voff, unsigned voff4, unsigned dst) {
  unsigned keep;
  if constexpr (NT) FILL8_BODY(" nt"); else FILL8_BODY("");
}
#define K_NT 0
#define V_NT 1
template <int PAD> __device__ __forceinline__ void fill8(const void* sbase, unsigned voff, unsigned voff4, unsigned dst) {
  fill8p<PAD, (PAD == 16) ? K_NT : V_NT>(sbase, voff, voff4, dst);
}
__device__ __forceinline__ void wpiece(const void* sbase, unsigned voff, unsigned dst) {
  unsigned keep;
  asm volatile("s_mov_b32 %0, m0\n\ts_mov_b32 m0, %3\n\ts_nop 0\n\tglobal_load_lds_dwordx4 %1, %2" W_AUX "\n\ts_mov_b32 m0, %0"
               : "=&s"(keep) : "v"(voff), "s"(sbase), "s"(dst) : "memory");
}
__device__ __forceinline__ void wpiece64(const void* vaddr, unsigned dst) {
  unsigned keep;
  asm volatile("s_mov_b32 %0, m0\n\ts_mov_b32 m0, %2\n\ts_nop 0\n\tglobal_load_lds_dwordx4 %1, off" W_AUX "\n\ts_mov_b32 m0, %0"
               : "=&s"(keep) : "v"(vaddr), "s"(dst) : "memory");
}
__device__ __forceinline__ bf16x8 trfrag(LAS unsigned char* p0, LAS unsigned char* p1) {
  const s16x4 a = __builtin_amdgcn_ds_read_tr16_b64_v4i16((LAS s16x4*)p0), b = __builtin_amdgcn_ds_read_tr16_b64_v4i16((LAS s16x4*)p1);
  return __builtin_bit_cast(bf16x8, __builtin_shufflevector(a, b, 0, 1, 2, 3, 4, 5, 6, 7));
}
__device__ __forceinline__ void put(gu64* p, unsigned tag, unsigned val) { __hip_atomic_store(p, ((u64)tag << 32) | val, RLX_AGENT); }
constexpr u64 SPIN_CAP = 5000000;
template <int N> __device__ __forceinline__ void sweep(const gu64* g, int stride, unsigned tag, unsigned (&v)[N]) {
  const u64 t0 = rt();
  for (;;) {
    bool ok = true;
#pragma unroll
    for (int k = 0; k < N; ++k) { const u64 x = __hip_atomic_load(g + k * stride, RLX_AGENT); v[k] = (unsigned)x; ok &= (unsigned)(x >> 32) == tag; }
    if (__all(ok)) return;
    if (rt() - t0 > SPIN_CAP) return;
    __builtin_amdgcn_s_sleep(1);
  }
}

constexpr int KPAD = 16, VPAD = 32, KBLK = 1024 + KPAD, VBLK = 1024 + VPAD, SLOT = 8 * VBLK;
constexpr int RING = 4 * SLOT;
constexpr int MRG_OFF = 4 * RING, MRG_REC = 132;
constexpr int QP_OFF = MRG_OFF, HP_OFF = MRG_OFF + 4096;
constexpr int STG_OFF = MRG_OFF + 4 * 4 * MRG_REC * 4;
constexpr int WD_OFF = STG_OFF;
constexpr int Q_OFF = STG_OFF + 16384;
constexpr int O_OFF = Q_OFF + 1024;
constexpr int TAG_OFF = O_OFF + 1024;
constexpr int LDS_BYTES = TAG_OFF + 16;
static_assert(LDS_BYTES <= 163840, "lds");

constexpr size_t WS_E = 0;
constexpr size_t WS_QG = 4096;
constexpr size_t WS_HG = WS_QG + 8 * 8 * 256 * 8;
constexpr size_t WS_DG = WS_HG + 65536;
constexpr size_t WS_PG = WS_DG + 32 * 8 * 8 * 16 * 8;
constexpr int PG_REC = 144;
constexpr size_t WS_AG = WS_PG + (size_t)256 * 4 * PG_REC * 8;
constexpr size_t WS_ST = WS_AG + (size_t)8 * 4 * 8 * 512 * 8;

struct Args { const unsigned char *x, *K, *V, *wq, *wo, *wg, *wu, *wd, *gw; unsigned char* ws; u16* out; };

#define ST(i) do {} while (0)
#define ST_FLUSH() do {} while (0)

__global__ void __launch_bounds__(320, 1) fused_kernel(Args a) {
  extern __shared__ __attribute__((aligned(16))) unsigned char lds_[];
  LAS unsigned char* lds = (LAS unsigned char*)lds_;
  const unsigned ldsb = (unsigned)(size_t)lds;
  const int tid = threadIdx.x, lane = tid & 63, wave = __builtin_amdgcn_readfirstlane(tid >> 6);
  const int bid = blockIdx.x, xg = bid & 7, idx = bid >> 3;
  const int kvh = xg, b = idx >> 2, sp = idx & 3;
  const int n = lane & 15, g = lane >> 4, q4 = n & 3, r4 = n >> 2;

  if (wave < 4) {
    constexpr int NU = 16;
    const unsigned ring = ldsb + wave * RING;
    LAS unsigned char* lring = lds + wave * RING;
    const int tbase = (8 * g + r4) * 32 + q4 * 8;
    const size_t wgbase = ((size_t)(b * HKV + kvh) * SK + (size_t)sp * 2048) * 256;
    const unsigned char* kb = a.K + wgbase + (size_t)wave * 8192;
    const unsigned char* vb = a.V + wgbase + (size_t)wave * 8192;
    const int wg = (b * HKV + kvh) * 4 + sp;
    const int rot = (wg * 5) % NU;
    const unsigned swz = (unsigned)((n ^ (g << 2)) * 16);
    const unsigned voff = (unsigned)(g * 256) + swz, voff4 = voff + 4096;
    auto uoff = [&](int u) { int t = u + rot; if (t >= NU) t -= NU; return (size_t)t * 32768; };
    const int kbase = r4 * KBLK + q4 * 256 + ((g ^ q4) * 64);
    int vbase[4];
#pragma unroll
    for (int e2 = 0; e2 < 4; ++e2) vbase[e2] = g * VBLK + r4 * 256 + ((e2 ^ r4) * 64) + (q4 >> 1) * 16 + (q4 & 1) * 8;
    auto readK = [&](int so, bf16x8 (&kf)[8]) {
#pragma unroll
      for (int i = 0; i < 8; ++i) kf[i] = *(const LAS bf16x8*)(lring + so + kbase + (i >> 2) * 4 * KBLK + (i & 3) * 16);
      asm volatile("s_waitcnt lgkmcnt(0)" : "+v"(kf[0]), "+v"(kf[1]), "+v"(kf[2]), "+v"(kf[3]), "+v"(kf[4]), "+v"(kf[5]), "+v"(kf[6]), "+v"(kf[7]) :: "memory");
    };
    auto readV = [&](int so, bf16x8 (&vf)[8]) {
#pragma unroll
      for (int e = 0; e < 8; ++e) { LAS unsigned char* p0 = lring + so + vbase[e >> 1] + (e & 1) * 32; vf[e] = trfrag(p0, p0 + 4 * VBLK); }
      asm volatile("s_waitcnt lgkmcnt(0)" : "+v"(vf[0]), "+v"(vf[1]), "+v"(vf[2]), "+v"(vf[3]), "+v"(vf[4]), "+v"(vf[5]), "+v"(vf[6]), "+v"(vf[7]) :: "memory");
    };
    fill8<KPAD>(kb + uoff(0), voff, voff4, ring);
    bf16x8 xf[4];
    const unsigned stg = ldsb + STG_OFF + wave * 4096;
    LAS unsigned char* lstg = lds + STG_OFF + wave * 4096;
    {
      const unsigned xo = (unsigned)((n & 7) * 1024 + wave * 256 + g * 16);
      asm volatile("global_load_dwordx4 %0, %4, %5\n\tglobal_load_dwordx4 %1, %4, %5 offset:64\n\tglobal_load_dwordx4 %2, %4, %5 offset:128\n\tglobal_load_dwordx4 %3, %4, %5 offset:192"
                   : "=&v"(xf[0]), "=&v"(xf[1]), "=&v"(xf[2]), "=&v"(xf[3]) : "v"(xo), "s"(a.x) : "memory");
      const unsigned voffw = (unsigned)((wave * 128 + (lane >> 1)) * 8192 + (lane & 1) * 16);
      const unsigned char* src = a.wq + (size_t)(kvh * 512 + idx * 16) * 2;
#pragma unroll
      for (int i = 0; i < 4; ++i) wpiece(src, voffw + i * 32 * 8192, stg + i * 1024);
    }
    fill8<VPAD>(vb + uoff(0), voff, voff4, ring + SLOT);
    asm volatile("s_waitcnt vmcnt(8)" : "+v"(xf[0]), "+v"(xf[1]), "+v"(xf[2]), "+v"(xf[3]) :: "memory");
    ST(1);
    auto proj = [&](int off) {
      f32x4 c = {0.f, 0.f, 0.f, 0.f};
      bf16x8 wf_[4];
#pragma unroll
      for (int i = 0; i < 4; ++i) wf_[i] = trfrag(lstg + i * 1024 + tbase, lstg + i * 1024 + tbase + 128);
#pragma unroll
      for (int i = 0; i < 4; ++i) c = __builtin_amdgcn_mfma_f32_16x16x32_bf16(wf_[i], xf[i], c, 0, 0, 0);
      *(LAS f32x4*)(lds + off + wave * 1024 + lane * 16) = c;
    };
    proj(QP_OFF);
    wg_barrier();
    {
      const int hpc = xg * 28 + (idx < 28 ? idx : 27);
      const unsigned char* src2 = ((lane & 1) ? a.wu : a.wg) + (size_t)(wave * 128 + (lane >> 1)) * (FF * 2) + (size_t)hpc * 16;
#pragma unroll
      for (int i = 0; i < 4; ++i) wpiece64(src2 + (size_t)i * 32 * FF * 2, stg + i * 1024);
    }
    fill8<KPAD>(kb + uoff(1), voff, voff4, ring + 2 * SLOT);
    wait_vm<8>();
    fill8<VPAD>(vb + uoff(1), voff, voff4, ring + 3 * SLOT);
    proj(HP_OFF);
    wg_barrier();
    bf16x8 k0f[8], v0f[8], k1f[8], v1f[8];
    readK(0, k0f);
    wait_vm<8>();
    fill8<KPAD>(kb + uoff(2), voff, voff4, ring);
    readV(SLOT, v0f);
    wait_vm<8>();
    fill8<VPAD>(vb + uoff(2), voff, voff4, ring + SLOT);
    readK(2 * SLOT, k1f);
    wait_vm<8>();
    fill8<KPAD>(kb + uoff(3), voff, voff4, ring + 2 * SLOT);
    readV(3 * SLOT, v1f);
    wait_vm<8>();
    fill8<VPAD>(vb + uoff(3), voff, voff4, ring + 3 * SLOT);
    ST(2);
    wg_barrier();
    bf16x8 qf[4];
#pragma unroll
    for (int t = 0; t < 4; ++t) qf[t] = *(const LAS bf16x8*)(lds + Q_OFF + q4 * 256 + g * 64 + t * 16);
    const unsigned tag = __builtin_amdgcn_readfirstlane(*(const LAS unsigned*)(lds + TAG_OFF));
    constexpr float C2 = 0.088388347648318440f * 1.4426950408889634f;
    const unsigned char* wob = a.wo + ((size_t)(kvh * 512 + wave * 128) * DM + sp * 128) * 2;
    const unsigned voffo = (unsigned)(g * 1024) + swz;
    auto fillwo = [&](int s, unsigned dst) {
#pragma unroll
      for (int i = 0; i < 8; ++i) wpiece(wob, voffo + (unsigned)(s * 32 + i * 4) * 1024, dst + i * VBLK);
    };
    f32x4 acc[8];
#pragma unroll
    for (int e = 0; e < 8; ++e) acc[e] = f32x4{0.f, 0.f, 0.f, 0.f};
    float m = -INFINITY, l = 0.f;
    auto scores = [&](const bf16x8 (&kf)[8]) -> bf16x8 {
      f32x4 s0 = {0.f, 0.f, 0.f, 0.f}, s1 = {0.f, 0.f, 0.f, 0.f};
#pragma unroll
      for (int t = 0; t < 4; ++t) {
        s0 = __builtin_amdgcn_mfma_f32_16x16x32_bf16(kf[t], qf[t], s0, 0, 0, 0);
        s1 = __builtin_amdgcn_mfma_f32_16x16x32_bf16(kf[4 + t], qf[t], s1, 0, 0, 0);
      }
      float s[8];
#pragma unroll
      for (int j = 0; j < 4; ++j) { s[j] = s0[j] * C2; s[4 + j] = s1[j] * C2; }
      float pm = fmaxf(fmaxf(fmaxf(s[0], s[1]), fmaxf(s[2], s[3])), fmaxf(fmaxf(s[4], s[5]), fmaxf(s[6], s[7])));
      pm = xrow_max(pm);
      if (__any(pm > m)) {
        const float mn = fmaxf(m, pm);
        const float alpha = __builtin_amdgcn_exp2f(m - mn);
        m = mn; l *= alpha;
        const float a0 = dpp<0x00>(alpha), a1 = dpp<0x55>(alpha), a2 = dpp<0xAA>(alpha), a3 = dpp<0xFF>(alpha);
#pragma unroll
        for (int e = 0; e < 8; ++e) { acc[e][0] *= a0; acc[e][1] *= a1; acc[e][2] *= a2; acc[e][3] *= a3; }
      }
      float p[8];
#pragma unroll
      for (int j = 0; j < 8; ++j) p[j] = __builtin_amdgcn_exp2f(s[j] - m);
      l += ((p[0] + p[1]) + (p[2] + p[3])) + ((p[4] + p[5]) + (p[6] + p[7]));
      const v4u pk = {cvtpk(p[0], p[1]), cvtpk(p[2], p[3]), cvtpk(p[4], p[5]), cvtpk(p[6], p[7])};
      return __builtin_bit_cast(bf16x8, pk);
    };
    auto pv = [&](const bf16x8 pf, const bf16x8 (&vf)[8]) {
#pragma unroll
      for (int e = 0; e < 8; ++e) acc[e] = __builtin_amdgcn_mfma_f32_16x16x32_bf16(pf, vf[e], acc[e], 0, 0, 0);
    };
    pv(scores(k0f), v0f);
    pv(scores(k1f), v1f);
#pragma unroll 2
    for (int u = 2; u < NU; ++u) {
      const int so = (u & 1) * 2 * SLOT;
      wait_vm<24>();
      bf16x8 kf[8];
      readK(so, kf);
      if (u + 2 < NU) fill8<KPAD>(kb + uoff(u + 2), voff, voff4, ring + so); else fillwo((u & 1) * 2, ring + so);
      const bf16x8 pf = scores(kf);
      wait_vm<24>();
      bf16x8 vf[8];
      readV(so + SLOT, vf);
      if (u + 2 < NU) fill8<VPAD>(vb + uoff(u + 2), voff, voff4, ring + so + SLOT); else fillwo((u & 1) * 2 + 1, ring + so + SLOT);
      pv(pf, vf);
    }
    ST(3);
    l = xrow_sum(l);
    LAS float* mrg = (LAS float*)(lds + MRG_OFF);
#pragma unroll
    for (int e = 0; e < 8; ++e) if ((e >> 1) == g) {
#pragma unroll
      for (int h = 0; h < 4; ++h) mrg[(wave * 4 + h) * MRG_REC + 4 + 16 * e + n] = acc[e][h];
    }
    if (lane < 4) { mrg[(wave * 4 + lane) * MRG_REC] = m; mrg[(wave * 4 + lane) * MRG_REC + 1] = l; }
    wg_barrier();
    const int d0 = 2 * lane;
    float M = -INFINITY;
#pragma unroll
    for (int w = 0; w < 4; ++w) M = fmaxf(M, mrg[(w * 4 + wave) * MRG_REC]);
    float L = 0.f, o0 = 0.f, o1 = 0.f;
#pragma unroll
    for (int w = 0; w < 4; ++w) {
      const LAS float* rec = mrg + (w * 4 + wave) * MRG_REC;
      const float wt = __builtin_amdgcn_exp2f(rec[0] - M);
      L = fmaf(wt, rec[1], L); o0 = fmaf(wt, rec[4 + d0], o0); o1 = fmaf(wt, rec[4 + d0 + 1], o1);
    }
    gu64* pg = (gu64*)(a.ws + WS_PG);
    {
      gu64* mine = pg + (size_t)(wg * 4 + wave) * PG_REC;
      put(mine + lane, tag, __float_as_uint(o0));
      put(mine + 64 + lane, tag, __float_as_uint(o1));
      if (lane < 2) put(mine + 128 + lane, tag, __float_as_uint(lane ? L : M));
    }
    ST(4);
    wait_vm<3>();
    bf16x8 wf[32];
#pragma unroll
    for (int s = 0; s < 4; ++s)
#pragma unroll
      for (int e = 0; e < 8; ++e) {
        LAS unsigned char* p0 = lring + s * SLOT + vbase[e >> 1] + (e & 1) * 32;
        wf[s * 8 + e] = trfrag(p0, p0 + 4 * VBLK);
      }
    ST(5);
    float pm_[4], pl_[4], p0_[4], p1_[4];
    {
      const gu64* grp = pg + (size_t)((wg & ~3) * 4 + wave) * PG_REC;
      const u64 t0 = rt();
      for (;;) {
        bool ok = true;
#pragma unroll
        for (int s = 0; s < 4; ++s) {
          const gu64* r = grp + (size_t)s * 4 * PG_REC;
          const u64 x0 = __hip_atomic_load(r + lane, RLX_AGENT), x1 = __hip_atomic_load(r + 64 + lane, RLX_AGENT);
          const u64 xm = __hip_atomic_load(r + 128, RLX_AGENT), xl = __hip_atomic_load(r + 129, RLX_AGENT);
          ok &= (unsigned)(x0 >> 32) == tag && (unsigned)(x1 >> 32) == tag && (unsigned)(xm >> 32) == tag && (unsigned)(xl >> 32) == tag;
          p0_[s] = __uint_as_float((unsigned)x0); p1_[s] = __uint_as_float((unsigned)x1); pm_[s] = __uint_as_float((unsigned)xm); pl_[s] = __uint_as_float((unsigned)xl);
        }
        if (__all(ok)) break;
        if (rt() - t0 > SPIN_CAP) break;
      }
    }
    ST(6);
    {
      const float MM = fmaxf(fmaxf(pm_[0], pm_[1]), fmaxf(pm_[2], pm_[3]));
      float LL = 0.f, y0 = 0.f, y1 = 0.f;
#pragma unroll
      for (int s = 0; s < 4; ++s) { const float wt = __builtin_amdgcn_exp2f(pm_[s] - MM); LL = fmaf(wt, pl_[s], LL); y0 = fmaf(wt, p0_[s], y0); y1 = fmaf(wt, p1_[s], y1); }
      const float inv = 1.f / LL;
      *(LAS unsigned*)(lds + O_OFF + wave * 256 + lane * 4) = cvtpk(y0 * inv, y1 * inv);
    }
    asm volatile("s_waitcnt lgkmcnt(0)" ::: "memory");
    f32x4 ac[8];
#pragma unroll
    for (int e = 0; e < 8; ++e) ac[e] = f32x4{0.f, 0.f, 0.f, 0.f};
#pragma unroll
    for (int s = 0; s < 4; ++s) {
      const v2u lo = *(const LAS v2u*)(lds + O_OFF + wave * 256 + s * 64 + g * 8), hi = *(const LAS v2u*)(lds + O_OFF + wave * 256 + s * 64 + 32 + g * 8);
      const v4u of = {lo.x, lo.y, hi.x, hi.y};
#pragma unroll
      for (int e = 0; e < 8; ++e) ac[e] = __builtin_amdgcn_mfma_f32_16x16x32_bf16(__builtin_bit_cast(bf16x8, of), wf[s * 8 + e], ac[e], 0, 0, 0);
    }
    {
      gu64* ag = (gu64*)(a.ws + WS_AG) + (size_t)((b * 4 + sp) * 8) * 512 + (kvh * 4 + wave) * 16 + n;
      const float v0 = g == 0 ? ac[0][0] : g == 1 ? ac[2][0] : g == 2 ? ac[4][0] : ac[6][0];
      const float v1 = g == 0 ? ac[1][0] : g == 1 ? ac[3][0] : g == 2 ? ac[5][0] : ac[7][0];
      put(ag + (size_t)(2 * g) * 512, tag, __float_as_uint(v0));
      put(ag + (size_t)(2 * g + 1) * 512, tag, __float_as_uint(v1));
    }
    ST(7);
    ST_FLUSH();
  } else {
    __builtin_amdgcn_s_setprio(2);
    const unsigned tag = __builtin_amdgcn_readfirstlane(__hip_atomic_load((const gu32*)(a.ws + WS_E), RLX_AGENT)) + 1u;
    const v4u xg8 = *(const GAS v4u*)(a.x + (size_t)b * 1024 + lane * 16), gw8 = *(const GAS v4u*)(a.gw + lane * 16);
    const int tbase = (8 * g + r4) * 32 + q4 * 8;
    float gate;
    {
      float s = 0.f;
#pragma unroll
      for (int k = 0; k < 4; ++k) {
        s += rbf(bf2f(xg8[k] & 0xffffu) * bf2f(gw8[k] & 0xffffu));
        s += rbf(bf2f(xg8[k] >> 16) * bf2f(gw8[k] >> 16));
      }
      s += dpp<0xB1>(s); s += dpp<0x4E>(s); s += dpp<0x141>(s); s += dpp<0x128>(s);
      s = xrow_sum(s);
      gate = rbf(sigmoidf_(rbf(s)));
    }
    if (lane == 0) *(LAS unsigned*)(lds + TAG_OFF) = tag;
    wg_barrier();
    {
      const LAS f32x4* qp = (const LAS f32x4*)(lds + QP_OFF) + lane;
      const f32x4 c0 = (qp[0] + qp[64]) + (qp[128] + qp[192]);
      const unsigned val = n < 8 ? cvtpk(c0[0], c0[1]) : cvtpk(c0[2], c0[3]);
      put((gu64*)(a.ws + WS_QG) + (size_t)(kvh * 8 + (n & 7)) * 256 + idx * 8 + g * 2 + (n >> 3), tag, val);
    }
    ST(1);
    wg_barrier();
    if (idx < 28) {
      const LAS f32x4* hp_ = (const LAS f32x4*)(lds + HP_OFF) + lane;
      const f32x4 c0 = (hp_[0] + hp_[64]) + (hp_[128] + hp_[192]);
      float hv[4];
#pragma unroll
      for (int j = 0; j < 4; ++j) {
        auto t = __builtin_amdgcn_permlane32_swap(__float_as_uint(c0[j]), __float_as_uint(c0[j]), false, false);
        hv[j] = rbf(sigmoidf_(rbf(__uint_as_float(t[0])))) * rbf(__uint_as_float(t[1]));
      }
      const unsigned val = n < 8 ? cvtpk(hv[0], hv[1]) : cvtpk(hv[2], hv[3]);
      if (g < 2) put((gu64*)(a.ws + WS_HG) + (size_t)((xg * 28 + idx) * 8 + (n & 7)) * 4 + g * 2 + (n >> 3), tag, val);
    }
    ST(2);
    {
      unsigned qv[4];
      sweep<4>((const gu64*)(a.ws + WS_QG) + (size_t)(kvh * 8 + b) * 256 + lane, 64, tag, qv);
#pragma unroll
      for (int k = 0; k < 4; ++k) *(LAS unsigned*)(lds + Q_OFF + (lane + 64 * k) * 4) = qv[k];
    }
    {
      const unsigned voffd = (unsigned)((lane >> 1) * 1024 + (lane & 1) * 16);
      const unsigned char* src = a.wd + ((size_t)(xg * 224) * DM + idx * 16) * 2;
#pragma unroll
      for (int i = 0; i < 7; ++i) wpiece(src, voffd + i * 32 * 1024, ldsb + WD_OFF + i * 1024);
    }
    unsigned hv[14];
    const gu64* hg = (const gu64*)(a.ws + WS_HG) + (size_t)((xg * 28 + g) * 8 + (n & 7)) * 4 + (n >> 3) * 2;
    u64 hx[14];
#pragma unroll
    for (int s = 0; s < 7; ++s) { hx[2 * s] = __hip_atomic_load(hg + s * 128, RLX_AGENT); hx[2 * s + 1] = __hip_atomic_load(hg + s * 128 + 1, RLX_AGENT); }
    ST(3);
    wg_barrier();
    __builtin_amdgcn_s_setprio(0);
    {
      const u64 t0 = rt();
      for (;;) {
        bool ok = true;
#pragma unroll
        for (int k = 0; k < 14; ++k) { hv[k] = (unsigned)hx[k]; ok &= (unsigned)(hx[k] >> 32) == tag; }
        if (__all(ok)) break;
        if (rt() - t0 > SPIN_CAP) break;
        __builtin_amdgcn_s_sleep(8);
#pragma unroll
        for (int s = 0; s < 7; ++s) { hx[2 * s] = __hip_atomic_load(hg + s * 128, RLX_AGENT); hx[2 * s + 1] = __hip_atomic_load(hg + s * 128 + 1, RLX_AGENT); }
      }
      wait_vm<0>();
      f32x4 c0 = {0.f, 0.f, 0.f, 0.f};
#pragma unroll
      for (int s = 0; s < 7; ++s) {
        const unsigned o0 = (unsigned)__builtin_amdgcn_mov_dpp((int)hv[2 * s], 0x128, 0xf, 0xf, true), o1 = (unsigned)__builtin_amdgcn_mov_dpp((int)hv[2 * s + 1], 0x128, 0xf, 0xf, true);
        const v4u hf = n < 8 ? v4u{hv[2 * s], hv[2 * s + 1], o0, o1} : v4u{o0, o1, hv[2 * s], hv[2 * s + 1]};
        c0 = __builtin_amdgcn_mfma_f32_16x16x32_bf16(__builtin_bit_cast(bf16x8, hf), trfrag(lds + WD_OFF + s * 1024 + tbase, lds + WD_OFF + s * 1024 + tbase + 128), c0, 0, 0, 0);
      }
      gu64* dg = (gu64*)(a.ws + WS_DG) + (size_t)(idx * 8 + (g & 1) * 4 + (g >> 1) * 2) * 128 + xg * 16 + n;
      put(dg, tag, __float_as_uint(g < 2 ? c0[0] : c0[2]));
      put(dg + 128, tag, __float_as_uint(g < 2 ? c0[1] : c0[3]));
    }
    ST(4);
    const int c16 = sp * 8 + kvh;
    wg_barrier();
    ST(5);
    float bm;
    {
      unsigned dv[2];
      sweep<2>((const gu64*)(a.ws + WS_DG) + (size_t)(c16 * 8 + b) * 128 + lane, 64, tag, dv);
      bm = rbf(xrow_sum(__uint_as_float(dv[0]) + __uint_as_float(dv[1])));
    }
    ST(6);
    {
      unsigned av[8];
      sweep<8>((const gu64*)(a.ws + WS_AG) + (size_t)((b * 4 + sp) * 8 + kvh) * 512 + lane, 64, tag, av);
      float s = 0.f;
#pragma unroll
      for (int k = 0; k < 8; ++k) s += __uint_as_float(av[k]);
      const float A = rbf(xrow_sum(s));
      const float r = rbf(gate * A) + rbf(rbf(1.f - gate) * bm);
      if (lane < 16) a.out[b * DM + c16 * 16 + lane] = (u16)(cvtpk(r, r) & 0xffffu);
      if (bid == 0 && lane == 0) __hip_atomic_store((gu32*)(a.ws + WS_E), tag, RLX_AGENT);
    }
    ST(7);
    ST_FLUSH();
  }
}

extern "C" void kernel_launch(void* const* d_in, const int* in_sizes, int n_in, void* d_out, int out_size, void* d_ws, size_t ws_size, hipStream_t stream) {
  Args a;
  a.x = (const unsigned char*)d_in[0]; a.K = (const unsigned char*)d_in[1]; a.V = (const unsigned char*)d_in[2];
  a.wq = (const unsigned char*)d_in[3]; a.wo = (const unsigned char*)d_in[4];
  a.wg = (const unsigned char*)d_in[6]; a.wu = (const unsigned char*)d_in[7]; a.wd = (const unsigned char*)d_in[8]; a.gw = (const unsigned char*)d_in[9];
  a.ws = (unsigned char*)d_ws; a.out = (u16*)d_out;
  (void)hipFuncSetAttribute((const void*)fused_kernel, hipFuncAttributeMaxDynamicSharedMemorySize, LDS_BYTES);
  fused_kernel<<<256, 320, LDS_BYTES, stream>>>(a);
}
```

```cpp
#include <hip/hip_runtime.h>
#include <hip/hip_bf16.h>
#include <cstdint>

#define GAS __attribute__((address_space(1)))
#define LAS __attribute__((address_space(3)))
typedef unsigned v4u __attribute__((ext_vector_type(4)));
typedef unsigned v2u __attribute__((ext_vector_type(2)));
typedef float f32x4 __attribute__((ext_vector_type(4)));
typedef __bf16 bf16x8 __attribute__((ext_vector_type(8)));
typedef short s16x4 __attribute__((ext_vector_type(4)));
typedef short s16x8 __attribute__((ext_vector_type(8)));
typedef unsigned short u16;
typedef unsigned long long u64;
typedef GAS u64 gu64;
typedef GAS unsigned gu32;
#define RLX_AGENT __ATOMIC_RELAXED, __HIP_MEMORY_SCOPE_AGENT

constexpr int NB = 8, DM = 512, HQ = 32, HKV = 8, SK = 8192, DH = 128, FF = 1792;
#define W_AUX ""

__device__ __forceinline__ float bf2f(unsigned v) { return __builtin_bit_cast(float, v << 16); }
__device__ __forceinline__ unsigned cvtpk(float lo, float hi) { unsigned r; asm("v_cvt_pk_bf16_f32 %0, %1, %2" : "=v"(r) : "v"(lo), "v"(hi)); return r; }
__device__ __forceinline__ float rbf(float x) { return __builtin_bit_cast(float, cvtpk(x, x) << 16); }
template <int CTRL> __device__ __forceinline__ float dpp(float x) {
  return __builtin_bit_cast(float, __builtin_amdgcn_mov_dpp(__builtin_bit_cast(int, x), CTRL, 0xf, 0xf, true));
}
__device__ __forceinline__ float xrow_max(float x) {
  auto s = __builtin_amdgcn_permlane16_swap(__float_as_uint(x), __float_as_uint(x), false, false);
  x = fmaxf(__uint_as_float(s[0]), __uint_as_float(s[1]));
  auto t = __builtin_amdgcn_permlane32_swap(__float_as_uint(x), __float_as_uint(x), false, false);
  return fmaxf(__uint_as_float(t[0]), __uint_as_float(t[1]));
}
__device__ __forceinline__ float xrow_sum(float x) {
  auto s = __builtin_amdgcn_permlane16_swap(__float_as_uint(x), __float_as_uint(x), false, false);
  x = __uint_as_float(s[0]) + __uint_as_float(s[1]);
  auto t = __builtin_amdgcn_permlane32_swap(__float_as_uint(x), __float_as_uint(x), false, false);
  return __uint_as_float(t[0]) + __uint_as_float(t[1]);
}
template <int N> __device__ __forceinline__ void wait_vm() { asm volatile("s_waitcnt vmcnt(%0)" ::"n"(N) : "memory"); }
__device__ __forceinline__ void wg_barrier() { asm volatile("s_waitcnt lgkmcnt(0)\n\ts_barrier" ::: "memory"); }
__device__ __forceinline__ u64 rt() { return __builtin_amdgcn_s_memrealtime(); }
__device__ __forceinline__ float sigmoidf_(float v) { return 1.f / (1.f + __expf(-v)); }

#define FILL8_BODY(KV_AUX) asm volatile( \
      "s_mov_b32 %0, m0\n\t" \
      "s_mov_b32 m0, %4\n\ts_nop 0\n\t" \
      "global_load_lds_dwordx4 %1, %3" KV_AUX "\n\t" \
      "s_add_u32 m0, m0, %5\n\ts_nop 0\n\t" \
      "global_load_lds_dwordx4 %1, %3 offset:1024" KV_AUX "\n\t" \
      "s_add_u32 m0, m0, %5\n\ts_nop 0\n\t" \
      "global_load_lds_dwordx4 %1, %3 offset:2048" KV_AUX "\n\t" \
      "s_add_u32 m0, m0, %5\n\ts_nop 0\n\t" \
      "global_load_lds_dwordx4 %1, %3 offset:3072" KV_AUX "\n\t" \
      "s_add_u32 m0, m0, %6\n\ts_nop 0\n\t" \
      "global_load_lds_dwordx4 %2, %3" KV_AUX "\n\t" \
      "s_add_u32 m0, m0, %5\n\ts_nop 0\n\t" \
      "global_load_lds_dwordx4 %2, %3 offset:1024" KV_AUX "\n\t" \
      "s_add_u32 m0, m0, %5\n\ts_nop 0\n\t" \
      "global_load_lds_dwordx4 %2, %3 offset:2048" KV_AUX "\n\t" \
      "s_add_u32 m0, m0, %5\n\ts_nop 0\n\t" \
      "global_load_lds_dwordx4 %2, %3 offset:3072" KV_AUX "\n\t" \
      "s_mov_b32 m0, %0" \
      : "=&s"(keep) \
      : "v"(voff), "v"(voff4), "s"(sbase), "s"(dst), "n"(PAD), "n"(4096 + PAD) \
      : "memory", "scc")
template <int PAD, int NT> __device__ __forceinline__ void fill8p(const void* sbase, unsigned voff, unsigned voff4, unsigned dst) {
  unsigned keep;
  if constexpr (NT) FILL8_BODY(" nt"); else FILL8_BODY("");
}
#define K_NT 1
#define V_NT 1
template <int PAD> __device__ __forceinline__ void fill8(const void* sbase, unsigned voff, unsigned voff4, unsigned dst) {
  fill8p<PAD, (PAD == 16) ? K_NT : V_NT>(sbase, voff, voff4, dst);
}
__device__ __forceinline__ void wpiece(const void* sbase, unsigned voff, unsigned dst) {
  unsigned keep;
  asm volatile("s_mov_b32 %0, m0\n\ts_mov_b32 m0, %3\n\ts_nop 0\n\tglobal_load_lds_dwordx4 %1, %2" W_AUX "\n\ts_mov_b32 m0, %0"
               : "=&s"(keep) : "v"(voff), "s"(sbase), "s"(dst) : "memory");
}
__device__ __forceinline__ void wpiece64(const void* vaddr, unsigned dst) {
  unsigned keep;
  asm volatile("s_mov_b32 %0, m0\n\ts_mov_b32 m0, %2\n\ts_nop 0\n\tglobal_load_lds_dwordx4 %1, off" W_AUX "\n\ts_mov_b32 m0, %0"
               : "=&s"(keep) : "v"(vaddr), "s"(dst) : "memory");
}
__device__ __forceinline__ bf16x8 trfrag(LAS unsigned char* p0, LAS unsigned char* p1) {
  const s16x4 a = __builtin_amdgcn_ds_read_tr16_b64_v4i16((LAS s16x4*)p0), b = __builtin_amdgcn_ds_read_tr16_b64_v4i16((LAS s16x4*)p1);
  return __builtin_bit_cast(bf16x8, __builtin_shufflevector(a, b, 0, 1, 2, 3, 4, 5, 6, 7));
}
__device__ __forceinline__ void put(gu64* p, unsigned tag, unsigned val) { __hip_atomic_store(p, ((u64)tag << 32) | val, RLX_AGENT); }
#define INFL 8
#define SLEEP0 16
#define SLEEP1 1
#define SLEEP2 1
#define MLP_DELAY 100
constexpr u64 SPIN_CAP = 5000000;
template <int N> __device__ __forceinline__ void sweep(const gu64* g, int stride, unsigned tag, unsigned (&v)[N]) {
  const u64 t0 = rt();
  for (;;) {
    bool ok = true;
#pragma unroll
    for (int k = 0; k < N; ++k) { const u64 x = __hip_atomic_load(g + k * stride, RLX_AGENT); v[k] = (unsigned)x; ok &= (unsigned)(x >> 32) == tag; }
    if (__all(ok)) return;
    if (rt() - t0 > SPIN_CAP) return;
    __builtin_amdgcn_s_sleep(1);
  }
}

constexpr int KPAD = 16, VPAD = 32, KBLK = 1024 + KPAD, VBLK = 1024 + VPAD, SLOT = 8 * VBLK;
constexpr int RING = 4 * SLOT;
constexpr int MRG_OFF = 4 * RING, MRG_REC = 132;
constexpr int Q_OFF = MRG_OFF + 4 * 4 * MRG_REC * 4;
constexpr int TAG_OFF = Q_OFF + 1024;
constexpr int T0_OFF = 0, T1_OFF = 65536, P_OFF = 131072, WD_OFF = P_OFF, O_OFF = P_OFF + 28672, O_PITCH = 272;
constexpr int LDS_BYTES = O_OFF + 8 * O_PITCH;
static_assert(LDS_BYTES <= 163840 && TAG_OFF + 16 <= LDS_BYTES, "lds");

constexpr int NSP = 3, PG_REC = 144;
constexpr size_t WS_E = 0;
constexpr size_t WS_QG = 4096;
constexpr size_t WS_HG = WS_QG + 8 * 8 * 256 * 8;
constexpr size_t WS_DG = WS_HG + 65536;
constexpr size_t WS_PG = WS_DG + 8 * 32 * 128 * 8;
constexpr size_t WS_AG = WS_PG + (size_t)32 * 8 * NSP * PG_REC * 8;
constexpr size_t WS_ST = WS_AG + (size_t)8 * 32 * 512 * 8;

struct Args { const unsigned char *x, *K, *V, *wq, *wo, *wg, *wu, *wd, *gw; unsigned char* ws; u16* out; };

#define ST(i) do {} while (0)
#define ST_FLUSH() do {} while (0)
#define NREG 3
#define PINFL 8
#define PAT 1

__global__ void __launch_bounds__(320, 1) fused_kernel(Args a) {
  extern __shared__ __attribute__((aligned(16))) unsigned char lds_[];
  LAS unsigned char* lds = (LAS unsigned char*)lds_;
  const unsigned ldsb = (unsigned)(size_t)lds;
  const int tid = threadIdx.x, lane = tid & 63, wave = __builtin_amdgcn_readfirstlane(tid >> 6);
  const int bid = blockIdx.x, xg = bid & 7, idx = bid >> 3;
  const int n = lane & 15, g = lane >> 4, q4 = n & 3, r4 = n >> 2;

  if (idx >= 8) {
    const int kvh = xg, b = (idx - 8) / NSP, sp = (idx - 8) % NSP;
    if (wave < 4) {
      const unsigned ring = ldsb + wave * RING;
      LAS unsigned char* lring = lds + wave * RING;
      const int pair = b * HKV + kvh;
      int u0, ustride, nu;
      if (PAT == 1) { const int lo = sp == 0 ? 0 : sp == 1 ? 86 : 171, cnt = sp == 0 ? 86 : 85; u0 = lo + wave; ustride = 4; nu = (cnt - wave + 3) >> 2; }
      else { const int gw = wave * 3 + sp; u0 = gw; ustride = 12; nu = gw < 4 ? 22 : 21; }
      const int rot = PAT == 1 ? ((pair * 3 + sp) * 5) % 21 : (pair * 5) % 21;
      const unsigned char* kb = a.K + (size_t)pair * (SK * 256) + (size_t)u0 * 8192;
      const unsigned char* vb = a.V + (size_t)pair * (SK * 256) + (size_t)u0 * 8192;
      const unsigned voff = (unsigned)(g * 256 + (n ^ (g << 2)) * 16), voff4 = voff + 4096;
      auto uoff = [&](int u) { int t = u + rot; if (t >= nu) t -= nu; return (size_t)(t * ustride) * 8192; };
      const int kbase = r4 * KBLK + q4 * 256 + ((g ^ q4) * 64);
      int vbase[4];
#pragma unroll
      for (int e2 = 0; e2 < 4; ++e2) vbase[e2] = g * VBLK + r4 * 256 + ((e2 ^ r4) * 64) + (q4 >> 1) * 16 + (q4 & 1) * 8;
      auto readK = [&](int so, bf16x8 (&kf)[8]) {
#pragma unroll
        for (int i = 0; i < 8; ++i) kf[i] = *(const LAS bf16x8*)(lring + so + kbase + (i >> 2) * 4 * KBLK + (i & 3) * 16);
        asm volatile("s_waitcnt lgkmcnt(0)" : "+v"(kf[0]), "+v"(kf[1]), "+v"(kf[2]), "+v"(kf[3]), "+v"(kf[4]), "+v"(kf[5]), "+v"(kf[6]), "+v"(kf[7]) :: "memory");
      };
      auto readV = [&](int so, bf16x8 (&vf)[8]) {
#pragma unroll
        for (int e = 0; e < 8; ++e) { LAS unsigned char* p0 = lring + so + vbase[e >> 1] + (e & 1) * 32; vf[e] = trfrag(p0, p0 + 4 * VBLK); }
        asm volatile("s_waitcnt lgkmcnt(0)" : "+v"(vf[0]), "+v"(vf[1]), "+v"(vf[2]), "+v"(vf[3]), "+v"(vf[4]), "+v"(vf[5]), "+v"(vf[6]), "+v"(vf[7]) :: "memory");
      };
      fill8<KPAD>(kb + uoff(0), voff, voff4, ring);
      fill8<VPAD>(vb + uoff(0), voff, voff4, ring + SLOT);
      if (PINFL < 16) wait_vm<PINFL>();
      ST(1);
      fill8<KPAD>(kb + uoff(1), voff, voff4, ring + 2 * SLOT);
      if (PINFL < 24) wait_vm<PINFL>();
      fill8<VPAD>(vb + uoff(1), voff, voff4, ring + 3 * SLOT);
      bf16x8 kr[NREG][8], vr[NREG][8];
#pragma unroll
      for (int u = 0; u < NREG; ++u) {
        const int so = (u & 1) * 2 * SLOT;
        wait_vm<PINFL>();
        readK(so, kr[u]);
        fill8<KPAD>(kb + uoff(u + 2), voff, voff4, ring + so);
        wait_vm<PINFL>();
        readV(so + SLOT, vr[u]);
        fill8<VPAD>(vb + uoff(u + 2), voff, voff4, ring + so + SLOT);
      }
      ST(2);
      wg_barrier();
      bf16x8 qf[4];
#pragma unroll
      for (int t = 0; t < 4; ++t) qf[t] = *(const LAS bf16x8*)(lds + Q_OFF + q4 * 256 + g * 64 + t * 16);
      const unsigned tag = __builtin_amdgcn_readfirstlane(*(const LAS unsigned*)(lds + TAG_OFF));
      constexpr float C2 = 0.088388347648318440f * 1.4426950408889634f;
      f32x4 acc[8];
#pragma unroll
      for (int e = 0; e < 8; ++e) acc[e] = f32x4{0.f, 0.f, 0.f, 0.f};
      float m = -INFINITY, l = 0.f;
      auto scores = [&](const bf16x8 (&kf)[8]) -> bf16x8 {
        f32x4 s0 = {0.f, 0.f, 0.f, 0.f}, s1 = {0.f, 0.f, 0.f, 0.f};
#pragma unroll
        for (int t = 0; t < 4; ++t) {
          s0 = __builtin_amdgcn_mfma_f32_16x16x32_bf16(kf[t], qf[t], s0, 0, 0, 0);
          s1 = __builtin_amdgcn_mfma_f32_16x16x32_bf16(kf[4 + t], qf[t], s1, 0, 0, 0);
        }
        float s[8];
#pragma unroll
        for (int j = 0; j < 4; ++j) { s[j] = s0[j] * C2; s[4 + j] = s1[j] * C2; }
        float pm = fmaxf(fmaxf(fmaxf(s[0], s[1]), fmaxf(s[2], s[3])), fmaxf(fmaxf(s[4], s[5]), fmaxf(s[6], s[7])));
        pm = xrow_max(pm);
        if (__any(pm > m)) {
          const float mn = fmaxf(m, pm);
          const float alpha = __builtin_amdgcn_exp2f(m - mn);
          m = mn; l *= alpha;
          const float a0 = dpp<0x00>(alpha), a1 = dpp<0x55>(alpha), a2 = dpp<0xAA>(alpha), a3 = dpp<0xFF>(alpha);
#pragma unroll
          for (int e = 0; e < 8; ++e) { acc[e][0] *= a0; acc[e][1] *= a1; acc[e][2] *= a2; acc[e][3] *= a3; }
        }
        float p[8];
#pragma unroll
        for (int j = 0; j < 8; ++j) p[j] = __builtin_amdgcn_exp2f(s[j] - m);
        l += ((p[0] + p[1]) + (p[2] + p[3])) + ((p[4] + p[5]) + (p[6] + p[7]));
        const v4u pk = {cvtpk(p[0], p[1]), cvtpk(p[2], p[3]), cvtpk(p[4], p[5]), cvtpk(p[6], p[7])};
        return __builtin_bit_cast(bf16x8, pk);
      };
      auto pv = [&](const bf16x8 pf, const bf16x8 (&vf)[8]) {
#pragma unroll
        for (int e = 0; e < 8; ++e) acc[e] = __builtin_amdgcn_mfma_f32_16x16x32_bf16(pf, vf[e], acc[e], 0, 0, 0);
      };
#pragma unroll
      for (int u = 0; u < NREG; ++u) pv(scores(kr[u]), vr[u]);
#pragma unroll 1
      for (int u = NREG; u < nu; ++u) {
        const int so = (u & 1) * 2 * SLOT;
        const bool more = u + 2 < nu;
        if (more) wait_vm<INFL>(); else if (u + 1 < nu) wait_vm<24>(); else wait_vm<8>();
        bf16x8 kf[8];
        readK(so, kf);
        if (more) fill8<KPAD>(kb + uoff(u + 2), voff, voff4, ring + so);
        const bf16x8 pf = scores(kf);
        if (more) wait_vm<INFL>(); else if (u + 1 < nu) wait_vm<16>(); else wait_vm<0>();
        bf16x8 vf[8];
        readV(so + SLOT, vf);
        if (more) fill8<VPAD>(vb + uoff(u + 2), voff, voff4, ring + so + SLOT);
        pv(pf, vf);
      }
      ST(3);
      l = xrow_sum(l);
      LAS float* mrg = (LAS float*)(lds + MRG_OFF);
#pragma unroll
      for (int e = 0; e < 8; ++e) if ((e >> 1) == g) {
#pragma unroll
        for (int h = 0; h < 4; ++h) mrg[(wave * 4 + h) * MRG_REC + 4 + 16 * e + n] = acc[e][h];
      }
      if (lane < 4) { mrg[(wave * 4 + lane) * MRG_REC] = m; mrg[(wave * 4 + lane) * MRG_REC + 1] = l; }
      wg_barrier();
      const int d0 = 2 * lane;
      float M = -INFINITY;
#pragma unroll
      for (int w = 0; w < 4; ++w) M = fmaxf(M, mrg[(w * 4 + wave) * MRG_REC]);
      float L = 0.f, o0 = 0.f, o1 = 0.f;
#pragma unroll
      for (int w = 0; w < 4; ++w) {
        const LAS float* rec = mrg + (w * 4 + wave) * MRG_REC;
        const float wt = __builtin_amdgcn_exp2f(rec[0] - M);
        L = fmaf(wt, rec[1], L); o0 = fmaf(wt, rec[4 + d0], o0); o1 = fmaf(wt, rec[4 + d0 + 1], o1);
      }
      gu64* mine = (gu64*)(a.ws + WS_PG) + (size_t)(((kvh * 4 + wave) * 8 + b) * NSP + sp) * PG_REC;
      put(mine + lane, tag, __float_as_uint(o0));
      put(mine + 64 + lane, tag, __float_as_uint(o1));
      if (lane < 2) put(mine + 128 + lane, tag, __float_as_uint(lane ? L : M));
      ST(4); ST(5); ST(6); ST(7);
      ST_FLUSH();
    } else {
      __builtin_amdgcn_s_setprio(3);
      const unsigned tag = __builtin_amdgcn_readfirstlane(__hip_atomic_load((const gu32*)(a.ws + WS_E), RLX_AGENT)) + 1u;
      if (lane == 0) *(LAS unsigned*)(lds + TAG_OFF) = tag;
      ST(1);
      unsigned qv[4];
      sweep<4>((const gu64*)(a.ws + WS_QG) + (size_t)(kvh * 8 + b) * 256 + lane, 64, tag, qv);
#pragma unroll
      for (int k = 0; k < 4; ++k) *(LAS unsigned*)(lds + Q_OFF + (lane + 64 * k) * 4) = qv[k];
      ST(2);
      wg_barrier();
      ST(3); ST(4); ST(5); ST(6); ST(7);
      ST_FLUSH();
    }
    return;
  }
  if (wave >= 4) { ST_FLUSH(); return; }
  const int j = idx;
  const int prow = lane >> 3;
  const int ce = (lane & 7) ^ (2 * ((lane >> 4) & 1)), co = ce ^ 4;
  const int fsw = ((r4 >> 1) & 1) | ((g & 1) << 1);
  const int trow = (8 * g + r4) * 128 + q4 * 8;
  auto tfrag = [&](LAS unsigned char* tile, int s, int t) { LAS unsigned char* p = tile + s * 4096 + trow + ((t ^ fsw) << 5); return trfrag(p, p + 512); };
  bf16x8 xf[4]; unsigned ev;
  {
    const unsigned xo = (unsigned)((n & 7) * 1024 + wave * 256 + g * 16), zero = 0;
    asm volatile("global_load_dword %4, %7, %8 sc1\n\tglobal_load_dwordx4 %0, %5, %6\n\tglobal_load_dwordx4 %1, %5, %6 offset:64\n\tglobal_load_dwordx4 %2, %5, %6 offset:128\n\tglobal_load_dwordx4 %3, %5, %6 offset:192"
                 : "=&v"(xf[0]), "=&v"(xf[1]), "=&v"(xf[2]), "=&v"(xf[3]), "=&v"(ev) : "v"(xo), "s"(a.x), "v"(zero), "s"(a.ws) : "memory");
  }
  {
    const unsigned char* src = a.wq + (size_t)(xg * 512 + j * 64) * 2;
    const unsigned ve = (unsigned)((wave * 128 + prow) * 8192 + ce * 16), vo = (unsigned)((wave * 128 + 8 + prow) * 8192 + co * 16);
#pragma unroll
    for (int i = 0; i < 8; ++i) { wpiece(src, ve + i * 16 * 8192, ldsb + T0_OFF + wave * 16384 + i * 2048); wpiece(src, vo + i * 16 * 8192, ldsb + T0_OFF + wave * 16384 + i * 2048 + 1024); }
  }
  asm volatile("s_waitcnt vmcnt(0)" : "+v"(xf[0]), "+v"(xf[1]), "+v"(xf[2]), "+v"(xf[3]), "+v"(ev) :: "memory");
  const unsigned tag = __builtin_amdgcn_readfirstlane(ev) + 1u;
  ST(1);
  auto proj = [&](LAS unsigned char* tile, LAS unsigned char* dst) {
    f32x4 c[4];
#pragma unroll
    for (int t = 0; t < 4; ++t) c[t] = f32x4{0.f, 0.f, 0.f, 0.f};
#pragma unroll
    for (int s = 0; s < 4; ++s)
#pragma unroll
      for (int t = 0; t < 4; ++t) c[t] = __builtin_amdgcn_mfma_f32_16x16x32_bf16(tfrag(tile, s, t), xf[s], c[t], 0, 0, 0);
#pragma unroll
    for (int t = 0; t < 4; ++t) *(LAS f32x4*)(dst + t * 1024 + lane * 16) = c[t];
  };
  proj(lds + T0_OFF + wave * 16384, lds + P_OFF + wave * 4096);
  wg_barrier();
  {
    const LAS f32x4* qp = (const LAS f32x4*)(lds + P_OFF + wave * 1024) + lane;
    const f32x4 c0 = (qp[0] + qp[256]) + (qp[512] + qp[768]);
    const unsigned val = n < 8 ? cvtpk(c0[0], c0[1]) : cvtpk(c0[2], c0[3]);
    put((gu64*)(a.ws + WS_QG) + (size_t)(xg * 8 + (n & 7)) * 256 + j * 32 + wave * 8 + g * 2 + (n >> 3), tag, val);
  }
  ST(2);
  {
    const int jc = j < 7 ? j : 6;
    const size_t col = (size_t)(xg * 224 + jc * 32) * 2;
    const unsigned char* se = (ce < 4 ? a.wg : a.wu) + (size_t)(wave * 128 + prow) * (FF * 2) + col + (ce & 3) * 16;
    const unsigned char* so = (co < 4 ? a.wg : a.wu) + (size_t)(wave * 128 + 8 + prow) * (FF * 2) + col + (co & 3) * 16;
#pragma unroll
    for (int i = 0; i < 8; ++i) { wpiece64(se + (size_t)i * 16 * FF * 2, ldsb + T1_OFF + wave * 16384 + i * 2048); wpiece64(so + (size_t)i * 16 * FF * 2, ldsb + T1_OFF + wave * 16384 + i * 2048 + 1024); }
  }
  wait_vm<0>();
  const int head = xg * 4 + (j >> 1);
  {
    const unsigned char* src = a.wo + ((size_t)(head * 128) * DM + (j & 1) * 256 + wave * 64) * 2;
    const unsigned ve = (unsigned)(prow * 1024 + ce * 16), vo = (unsigned)((8 + prow) * 1024 + co * 16);
#pragma unroll
    for (int i = 0; i < 8; ++i) { wpiece(src, ve + i * 16 * 1024, ldsb + T0_OFF + wave * 16384 + i * 2048); wpiece(src, vo + i * 16 * 1024, ldsb + T0_OFF + wave * 16384 + i * 2048 + 1024); }
  }
  proj(lds + T1_OFF + wave * 16384, lds + T1_OFF + wave * 16384);
  wg_barrier();
  {
    const unsigned char* src = a.wd + ((size_t)(xg * 224) * DM + j * 64) * 2;
#pragma unroll
    for (int i = 0; i < 7; ++i) { const int pc = wave * 7 + i; wpiece(src, (unsigned)((pc * 8 + prow) * 1024 + ((pc & 1) ? co : ce) * 16), ldsb + WD_OFF + pc * 1024); }
  }
  if (j < 7 && wave < 2) {
    const LAS f32x4* gp = (const LAS f32x4*)(lds + T1_OFF + wave * 1024) + lane;
    const f32x4 cg = (gp[0] + gp[1024]) + (gp[2048] + gp[3072]);
    const f32x4 cu = (gp[128] + gp[1024 + 128]) + (gp[2048 + 128] + gp[3072 + 128]);
    float hv[4];
#pragma unroll
    for (int k = 0; k < 4; ++k) hv[k] = rbf(sigmoidf_(rbf(cg[k]))) * rbf(cu[k]);
    const unsigned val = n < 8 ? cvtpk(hv[0], hv[1]) : cvtpk(hv[2], hv[3]);
    put((gu64*)(a.ws + WS_HG) + (size_t)(xg * 8 + (n & 7)) * 112 + j * 16 + wave * 8 + g * 2 + (n >> 3), tag, val);
  }
  float gate;
  {
    const v4u xg8 = *(const GAS v4u*)(a.x + (size_t)j * 1024 + lane * 16), gw8 = *(const GAS v4u*)(a.gw + lane * 16);
    float s = 0.f;
#pragma unroll
    for (int k = 0; k < 4; ++k) {
      s += rbf(bf2f(xg8[k] & 0xffffu) * bf2f(gw8[k] & 0xffffu));
      s += rbf(bf2f(xg8[k] >> 16) * bf2f(gw8[k] >> 16));
    }
    s += dpp<0xB1>(s); s += dpp<0x4E>(s); s += dpp<0x141>(s); s += dpp<0x128>(s);
    s = xrow_sum(s);
    gate = rbf(sigmoidf_(rbf(s)));
  }
  wait_vm<0>();
  wg_barrier();
  bf16x8 wf[16];
#pragma unroll
  for (int s = 0; s < 4; ++s)
#pragma unroll
    for (int t = 0; t < 4; ++t) wf[s * 4 + t] = tfrag(lds + T0_OFF + wave * 16384, s, t);
  {
    unsigned hv[14];
    const gu64* hg = (const gu64*)(a.ws + WS_HG) + (size_t)(xg * 8 + (n & 7)) * 112 + g * 4 + (n >> 3) * 2;
    const u64 t0 = rt();
    for (;;) {
      bool ok = true;
#pragma unroll
      for (int s = 0; s < 7; ++s) {
        const u64 y0 = __hip_atomic_load(hg + s * 16, RLX_AGENT), y1 = __hip_atomic_load(hg + s * 16 + 1, RLX_AGENT);
        hv[2 * s] = (unsigned)y0; hv[2 * s + 1] = (unsigned)y1; ok &= (unsigned)(y0 >> 32) == tag && (unsigned)(y1 >> 32) == tag;
      }
      if (__all(ok)) break;
      if (rt() - t0 > SPIN_CAP) break;
      __builtin_amdgcn_s_sleep(8);
    }
    f32x4 c0 = {0.f, 0.f, 0.f, 0.f};
#pragma unroll
    for (int s = 0; s < 7; ++s) {
      const unsigned o0 = (unsigned)__builtin_amdgcn_mov_dpp((int)hv[2 * s], 0x128, 0xf, 0xf, true), o1 = (unsigned)__builtin_amdgcn_mov_dpp((int)hv[2 * s + 1], 0x128, 0xf, 0xf, true);
      const v4u hf = n < 8 ? v4u{hv[2 * s], hv[2 * s + 1], o0, o1} : v4u{o0, o1, hv[2 * s], hv[2 * s + 1]};
      c0 = __builtin_amdgcn_mfma_f32_16x16x32_bf16(__builtin_bit_cast(bf16x8, hf), tfrag(lds + WD_OFF, s, wave), c0, 0, 0, 0);
    }
    gu64* dg = (gu64*)(a.ws + WS_DG) + (size_t)(((g & 1) * 4 + (g >> 1) * 2) * 32 + j * 4 + wave) * 128 + xg * 16 + n;
    put(dg, tag, __float_as_uint(g < 2 ? c0[0] : c0[2]));
    put(dg + 32 * 128, tag, __float_as_uint(g < 2 ? c0[1] : c0[3]));
  }
  ST(3);
  const int c16 = xg * 4 + wave;
  float bm;
  {
    unsigned dv[2];
    sweep<2>((const gu64*)(a.ws + WS_DG) + (size_t)(j * 32 + c16) * 128 + lane, 64, tag, dv);
    bm = rbf(xrow_sum(__uint_as_float(dv[0]) + __uint_as_float(dv[1])));
  }
  ST(4);
  {
    const gu64* grp = (const gu64*)(a.ws + WS_PG) + (size_t)((head * 8 + wave * 2) * NSP) * PG_REC;
    const u64 t0 = rt();
    for (;;) {
      const u64 y = __hip_atomic_load(grp + (size_t)(lane < 6 ? lane : 0) * PG_REC + 128, RLX_AGENT);
      if (__any((unsigned)(y >> 32) == tag)) break;
      if (rt() - t0 > SPIN_CAP) break;
      __builtin_amdgcn_s_sleep(SLEEP0);
    }
    float pm_[6], pl_[6], p0_[6], p1_[6];
    unsigned missing = 63u;
    for (;;) {
      u64 x0[6], x1[6], xm[6], xl[6];
#pragma unroll
      for (int s = 0; s < 6; ++s) if (missing & (1u << s)) {
        const gu64* r = grp + (size_t)s * PG_REC;
        x0[s] = __hip_atomic_load(r + lane, RLX_AGENT); x1[s] = __hip_atomic_load(r + 64 + lane, RLX_AGENT);
        xm[s] = __hip_atomic_load(r + 128, RLX_AGENT); xl[s] = __hip_atomic_load(r + 129, RLX_AGENT);
      }
#pragma unroll
      for (int s = 0; s < 6; ++s) if (missing & (1u << s)) {
        const bool ok = (unsigned)(x0[s] >> 32) == tag && (unsigned)(x1[s] >> 32) == tag && (unsigned)(xm[s] >> 32) == tag && (unsigned)(xl[s] >> 32) == tag;
        if (__all(ok)) {
          p0_[s] = __uint_as_float((unsigned)x0[s]); p1_[s] = __uint_as_float((unsigned)x1[s]); pm_[s] = __uint_as_float((unsigned)xm[s]); pl_[s] = __uint_as_float((unsigned)xl[s]);
          missing &= ~(1u << s);
        }
      }
      if (!missing) break;
      if (rt() - t0 > SPIN_CAP) break;
      __builtin_amdgcn_s_sleep(SLEEP1);
    }
    ST(5);
#pragma unroll
    for (int bb = 0; bb < 2; ++bb) {
      const float MM = fmaxf(fmaxf(pm_[3 * bb], pm_[3 * bb + 1]), pm_[3 * bb + 2]);
      float LL = 0.f, y0 = 0.f, y1 = 0.f;
#pragma unroll
      for (int s = 3 * bb; s < 3 * bb + 3; ++s) { const float wt = __builtin_amdgcn_exp2f(pm_[s] - MM); LL = fmaf(wt, pl_[s], LL); y0 = fmaf(wt, p0_[s], y0); y1 = fmaf(wt, p1_[s], y1); }
      const float inv = __builtin_amdgcn_rcpf(LL);
      *(LAS unsigned*)(lds + O_OFF + (wave * 2 + bb) * O_PITCH + lane * 4) = cvtpk(y0 * inv, y1 * inv);
    }
  }
  wg_barrier();
  {
    f32x4 c[4];
#pragma unroll
    for (int t = 0; t < 4; ++t) c[t] = f32x4{0.f, 0.f, 0.f, 0.f};
    bf16x8 of[4];
#pragma unroll
    for (int s = 0; s < 4; ++s) of[s] = *(const LAS bf16x8*)(lds + O_OFF + (n & 7) * O_PITCH + s * 64 + g * 16);
#pragma unroll
    for (int s = 0; s < 4; ++s)
#pragma unroll
      for (int t = 0; t < 4; ++t) c[t] = __builtin_amdgcn_mfma_f32_16x16x32_bf16(of[s], wf[s * 4 + t], c[t], 0, 0, 0);
    gu64* ag = (gu64*)(a.ws + WS_AG) + (size_t)(((g & 1) * 4 + (g >> 1) * 2) * 32 + (j & 1) * 16 + wave * 4) * 512 + head * 16 + n;
#pragma unroll
    for (int t = 0; t < 4; ++t) {
      put(ag + (size_t)t * 512, tag, __float_as_uint(g < 2 ? c[t][0] : c[t][2]));
      put(ag + (size_t)(32 + t) * 512, tag, __float_as_uint(g < 2 ? c[t][1] : c[t][3]));
    }
  }
  ST(6);
  {
    const gu64* ag = (const gu64*)(a.ws + WS_AG) + (size_t)(j * 32 + c16) * 512 + lane;
    unsigned av[8];
    unsigned missing = 255u;
    const u64 t0 = rt();
    for (;;) {
      u64 y[8];
#pragma unroll
      for (int k = 0; k < 8; ++k) if (missing & (1u << k)) y[k] = __hip_atomic_load(ag + k * 64, RLX_AGENT);
#pragma unroll
      for (int k = 0; k < 8; ++k) if (missing & (1u << k)) { if (__all((unsigned)(y[k] >> 32) == tag)) { av[k] = (unsigned)y[k]; missing &= ~(1u << k); } }
      if (!missing) break;
      if (rt() - t0 > SPIN_CAP) break;
      __builtin_amdgcn_s_sleep(SLEEP2);
    }
    float s = 0.f;
#pragma unroll
    for (int k = 0; k < 8; ++k) s += __uint_as_float(av[k]);
    const float A = rbf(xrow_sum(s));
    const float r = rbf(gate * A) + rbf(rbf(1.f - gate) * bm);
    if (lane < 16) a.out[j * DM + c16 * 16 + lane] = (u16)(cvtpk(r, r) & 0xffffu);
    if (bid == 0 && wave == 0 && lane == 0) __hip_atomic_store((gu32*)(a.ws + WS_E), tag, RLX_AGENT);
  }
  ST(7);
  ST_FLUSH();
}

extern "C" void kernel_launch(void* const* d_in, const int* in_sizes, int n_in, void* d_out, int out_size, void* d_ws, size_t ws_size, hipStream_t stream) {
  Args a;
  a.x = (const unsigned char*)d_in[0]; a.K = (const unsigned char*)d_in[1]; a.V = (const unsigned char*)d_in[2];
  a.wq = (const unsigned char*)d_in[3]; a.wo = (const unsigned char*)d_in[4];
  a.wg = (const unsigned char*)d_in[6]; a.wu = (const unsigned char*)d_in[7]; a.wd = (const unsigned char*)d_in[8]; a.gw = (const unsigned char*)d_in[9];
  a.ws = (unsigned char*)d_ws; a.out = (u16*)d_out;
  (void)hipFuncSetAttribute((const void*)fused_kernel, hipFuncAttributeMaxDynamicSharedMemorySize, LDS_BYTES);
  fused_kernel<<<256, 320, LDS_BYTES, stream>>>(a);
}
```

```cpp
#include <hip/hip_runtime.h>
#include <hip/hip_bf16.h>
#include <cstdint>

#define GAS __attribute__((address_space(1)))
#define LAS __attribute__((address_space(3)))
typedef unsigned v4u __attribute__((ext_vector_type(4)));
typedef unsigned v2u __attribute__((ext_vector_type(2)));
typedef float f32x4 __attribute__((ext_vector_type(4)));
typedef __bf16 bf16x8 __attribute__((ext_vector_type(8)));
typedef short s16x4 __attribute__((ext_vector_type(4)));
typedef short s16x8 __attribute__((ext_vector_type(8)));
typedef unsigned short u16;
typedef unsigned long long u64;
typedef GAS u64 gu64;
typedef GAS unsigned gu32;
#define RLX_AGENT __ATOMIC_RELAXED, __HIP_MEMORY_SCOPE_AGENT

constexpr int NB = 8, DM = 512, HQ = 32, HKV = 8, SK = 8192, DH = 128, FF = 1792;
#define W_AUX ""

__device__ __forceinline__ float bf2f(unsigned v) { return __builtin_bit_cast(float, v << 16); }
__device__ __forceinline__ unsigned cvtpk(float lo, float hi) { unsigned r; asm("v_cvt_pk_bf16_f32 %0, %1, %2" : "=v"(r) : "v"(lo), "v"(hi)); return r; }
__device__ __forceinline__ float rbf(float x) { return __builtin_bit_cast(float, cvtpk(x, x) << 16); }
template <int CTRL> __device__ __forceinline__ float dpp(float x) {
  return __builtin_bit_cast(float, __builtin_amdgcn_mov_dpp(__builtin_bit_cast(int, x), CTRL, 0xf, 0xf, true));
}
__device__ __forceinline__ float xrow_max(float x) {
  auto s = __builtin_amdgcn_permlane16_swap(__float_as_uint(x), __float_as_uint(x), false, false);
  x = fmaxf(__uint_as_float(s[0]), __uint_as_float(s[1]));
  auto t = __builtin_amdgcn_permlane32_swap(__float_as_uint(x), __float_as_uint(x), false, false);
  return fmaxf(__uint_as_float(t[0]), __uint_as_float(t[1]));
}
__device__ __forceinline__ float xrow_sum(float x) {
  auto s = __builtin_amdgcn_permlane16_swap(__float_as_uint(x), __float_as_uint(x), false, false);
  x = __uint_as_float(s[0]) + __uint_as_float(s[1]);
  auto t = __builtin_amdgcn_permlane32_swap(__float_as_uint(x), __float_as_uint(x), false, false);
  return __uint_as_float(t[0]) + __uint_as_float(t[1]);
}
template <int N> __device__ __forceinline__ void wait_vm() { asm volatile("s_waitcnt vmcnt(%0)" ::"n"(N) : "memory"); }
__device__ __forceinline__ void wg_barrier() { asm volatile("s_waitcnt lgkmcnt(0)\n\ts_barrier" ::: "memory"); }
__device__ __forceinline__ u64 rt() { return __builtin_amdgcn_s_memrealtime(); }
__device__ __forceinline__ float sigmoidf_(float v) { return 1.f / (1.f + __expf(-v)); }

#define FILL8_BODY(KV_AUX) asm volatile( \
      "s_mov_b32 %0, m0\n\t" \
      "s_mov_b32 m0, %4\n\ts_nop 0\n\t" \
      "global_load_lds_dwordx4 %1, %3" KV_AUX "\n\t" \
      "s_add_u32 m0, m0, %5\n\ts_nop 0\n\t" \
      "global_load_lds_dwordx4 %1, %3 offset:1024" KV_AUX "\n\t" \
      "s_add_u32 m0, m0, %5\n\ts_nop 0\n\t" \
      "global_load_lds_dwordx4 %1, %3 offset:2048" KV_AUX "\n\t" \
      "s_add_u32 m0, m0, %5\n\ts_nop 0\n\t" \
      "global_load_lds_dwordx4 %1, %3 offset:3072" KV_AUX "\n\t" \
      "s_add_u32 m0, m0, %6\n\ts_nop 0\n\t" \
      "global_load_lds_dwordx4 %2, %3" KV_AUX "\n\t" \
      "s_add_u32 m0, m0, %5\n\ts_nop 0\n\t" \
      "global_load_lds_dwordx4 %2, %3 offset:1024" KV_AUX "\n\t" \
      "s_add_u32 m0, m0, %5\n\ts_nop 0\n\t" \
      "global_load_lds_dwordx4 %2, %3 offset:2048" KV_AUX "\n\t" \
      "s_add_u32 m0, m0, %5\n\ts_nop 0\n\t" \
      "global_load_lds_dwordx4 %2, %3 offset:3072" KV_AUX "\n\t" \
      "s_mov_b32 m0, %0" \
      : "=&s"(keep) \
      : "v"(voff), "v"(voff4), "s"(sbase), "s"(dst), "n"(PAD), "n"(4096 + PAD) \
      : "memory", "scc")
template <int PAD, int NT> __device__ __forceinline__ void fill8p(const void* sbase, unsigned voff, unsigned voff4, unsigned dst) {
  unsigned keep;
  if constexpr (NT) FILL8_BODY(" nt"); else FILL8_BODY("");
}
#define K_NT 1
#define V_NT 1
template <int PAD> __device__ __forceinline__ void fill8(const void* sbase, unsigned voff, unsigned voff4, unsigned dst) {
  fill8p<PAD, (PAD == 16) ? K_NT : V_NT>(sbase, voff, voff4, dst);
}
__device__ __forceinline__ void wpiece(const void* sbase, unsigned voff, unsigned dst) {
  unsigned keep;
  asm volatile("s_mov_b32 %0, m0\n\ts_mov_b32 m0, %3\n\ts_nop 0\n\tglobal_load_lds_dwordx4 %1, %2" W_AUX "\n\ts_mov_b32 m0, %0"
               : "=&s"(keep) : "v"(voff), "s"(sbase), "s"(dst) : "memory");
}
__device__ __forceinline__ void wpiece64(const void* vaddr, unsigned dst) {
  unsigned keep;
  asm volatile("s_mov_b32 %0, m0\n\ts_mov_b32 m0, %2\n\ts_nop 0\n\tglobal_load_lds_dwordx4 %1, off" W_AUX "\n\ts_mov_b32 m0, %0"
               : "=&s"(keep) : "v"(vaddr), "s"(dst) : "memory");
}
__device__ __forceinline__ bf16x8 trfrag(LAS unsigned char* p0, LAS unsigned char* p1) {
  const s16x4 a = __builtin_amdgcn_ds_read_tr16_b64_v4i16((LAS s16x4*)p0), b = __builtin_amdgcn_ds_read_tr16_b64_v4i16((LAS s16x4*)p1);
  return __builtin_bit_cast(bf16x8, __builtin_shufflevector(a, b, 0, 1, 2, 3, 4, 5, 6, 7));
}
__device__ __forceinline__ void put(gu64* p, unsigned tag, unsigned val) { __hip_atomic_store(p, ((u64)tag << 32) | val, RLX_AGENT); }
#define INFL 8
#define SLEEP0 16
#define SLEEP1 1
#define SLEEP2 1
#define MLP_DELAY 100
constexpr u64 SPIN_CAP = 5000000;
template <int N> __device__ __forceinline__ void sweep(const gu64* g, int stride, unsigned tag, unsigned (&v)[N]) {
  const u64 t0 = rt();
  for (;;) {
    bool ok = true;
#pragma unroll
    for (int k = 0; k < N; ++k) { const u64 x = __hip_atomic_load(g + k * stride, RLX_AGENT); v[k] = (unsigned)x; ok &= (unsigned)(x >> 32) == tag; }
    if (__all(ok)) return;
    if (rt() - t0 > SPIN_CAP) return;
    __builtin_amdgcn_s_sleep(1);
  }
}

constexpr int KPAD = 16, VPAD = 32, KBLK = 1024 + KPAD, VBLK = 1024 + VPAD, SLOT = 8 * VBLK;
constexpr int RING = 4 * SLOT;
constexpr int MRG_OFF = 4 * RING, MRG_REC = 132;
constexpr int Q_OFF = MRG_OFF + 4 * 4 * MRG_REC * 4;
constexpr int TAG_OFF = Q_OFF + 1024;
constexpr int T0_OFF = 0, T1_OFF = 65536, P_OFF = 131072, WD_OFF = P_OFF, O_OFF = P_OFF + 28672, O_PITCH = 272;
constexpr int LDS_BYTES = O_OFF + 8 * O_PITCH;
static_assert(LDS_BYTES <= 163840 && TAG_OFF + 16 <= LDS_BYTES, "lds");

constexpr int NSP = 3, PG_REC = 144;
constexpr size_t WS_E = 0;
constexpr size_t WS_QG = 4096;
constexpr size_t WS_HG = WS_QG + 8 * 8 * 256 * 8;
constexpr size_t WS_DG = WS_HG + 65536;
constexpr size_t WS_PG = WS_DG + 8 * 32 * 128 * 8;
constexpr size_t WS_AG = WS_PG + (size_t)32 * 8 * NSP * PG_REC * 8;
constexpr size_t WS_ST = WS_AG + (size_t)8 * 32 * 512 * 8;

struct Args { const unsigned char *x, *K, *V, *wq, *wo, *wg, *wu, *wd, *gw; unsigned char* ws; u16* out; };

#define ST(i) do {} while (0)
#define ST_FLUSH() do {} while (0)
#define NREG 3
#define PINFL 8
#define PAT 1

__global__ void __launch_bounds__(320, 1) fused_kernel(Args a_) {
  u64 tstart_; const __attribute__((address_space(4))) Args* ap_ = (const __attribute__((address_space(4))) Args*)__builtin_amdgcn_kernarg_segment_ptr();
  asm volatile("s_memrealtime %0\n\ts_waitcnt lgkmcnt(0)" : "=s"(tstart_), "+s"(ap_) :: "memory");
  Args a; a.x = ap_->x; a.K = ap_->K; a.V = ap_->V; a.wq = ap_->wq; a.wo = ap_->wo; a.wg = ap_->wg; a.wu = ap_->wu; a.wd = ap_->wd; a.gw = ap_->gw; a.ws = ap_->ws; a.out = ap_->out;
  extern __shared__ __attribute__((aligned(16))) unsigned char lds_[];
  LAS unsigned char* lds = (LAS unsigned char*)lds_;
  const unsigned ldsb = (unsigned)(size_t)lds;
  const int tid = threadIdx.x, lane = tid & 63, wave = __builtin_amdgcn_readfirstlane(tid >> 6);
  const int bid = blockIdx.x, xg = bid & 7, idx = bid >> 3;
  const int n = lane & 15, g = lane >> 4, q4 = n & 3, r4 = n >> 2;

  if (idx >= 8) {
    const int kvh = xg, b = (idx - 8) / NSP, sp = (idx - 8) % NSP;
    if (wave < 4) {
      const unsigned ring = ldsb + wave * RING;
      LAS unsigned char* lring = lds + wave * RING;
      const int pair = b * HKV + kvh;
      int u0, ustride, nu;
      if (PAT == 1) { const int lo = sp == 0 ? 0 : sp == 1 ? 86 : 171, cnt = sp == 0 ? 86 : 85; u0 = lo + wave; ustride = 4; nu = (cnt - wave + 3) >> 2; }
      else { const int gw = wave * 3 + sp; u0 = gw; ustride = 12; nu = gw < 4 ? 22 : 21; }
      const int rot = PAT == 1 ? ((pair * 3 + sp) * 5) % 21 : (pair * 5) % 21;
      const unsigned char* kb = a.K + (size_t)pair * (SK * 256) + (size_t)u0 * 8192;
      const unsigned char* vb = a.V + (size_t)pair * (SK * 256) + (size_t)u0 * 8192;
      const unsigned voff = (unsigned)(g * 256 + (n ^ (g << 2)) * 16), voff4 = voff + 4096;
      auto uoff = [&](int u) { int t = u + rot; if (t >= nu) t -= nu; return (size_t)(t * ustride) * 8192; };
      const int kbase = r4 * KBLK + q4 * 256 + ((g ^ q4) * 64);
      int vbase[4];
#pragma unroll
      for (int e2 = 0; e2 < 4; ++e2) vbase[e2] = g * VBLK + r4 * 256 + ((e2 ^ r4) * 64) + (q4 >> 1) * 16 + (q4 & 1) * 8;
      auto readK = [&](int so, bf16x8 (&kf)[8]) {
#pragma unroll
        for (int i = 0; i < 8; ++i) kf[i] = *(const LAS bf16x8*)(lring + so + kbase + (i >> 2) * 4 * KBLK + (i & 3) * 16);
        asm volatile("s_waitcnt lgkmcnt(0)" : "+v"(kf[0]), "+v"(kf[1]), "+v"(kf[2]), "+v"(kf[3]), "+v"(kf[4]), "+v"(kf[5]), "+v"(kf[6]), "+v"(kf[7]) :: "memory");
      };
      auto readV = [&](int so, bf16x8 (&vf)[8]) {
#pragma unroll
        for (int e = 0; e < 8; ++e) { LAS unsigned char* p0 = lring + so + vbase[e >> 1] + (e & 1) * 32; vf[e] = trfrag(p0, p0 + 4 * VBLK); }
        asm volatile("s_waitcnt lgkmcnt(0)" : "+v"(vf[0]), "+v"(vf[1]), "+v"(vf[2]), "+v"(vf[3]), "+v"(vf[4]), "+v"(vf[5]), "+v"(vf[6]), "+v"(vf[7]) :: "memory");
      };
      fill8<KPAD>(kb + uoff(0), voff, voff4, ring);
      fill8<VPAD>(vb + uoff(0), voff, voff4, ring + SLOT);
      if (PINFL < 16) wait_vm<PINFL>();
      ST(1);
      fill8<KPAD>(kb + uoff(1), voff, voff4, ring + 2 * SLOT);
      if (PINFL < 24) wait_vm<PINFL>();
      fill8<VPAD>(vb + uoff(1), voff, voff4, ring + 3 * SLOT);
      bf16x8 kr[NREG][8], vr[NREG][8];
#pragma unroll
      for (int u = 0; u < NREG; ++u) {
        const int so = (u & 1) * 2 * SLOT;
        wait_vm<PINFL>();
        readK(so, kr[u]);
        fill8<KPAD>(kb + uoff(u + 2), voff, voff4, ring + so);
        wait_vm<PINFL>();
        readV(so + SLOT, vr[u]);
        fill8<VPAD>(vb + uoff(u + 2), voff, voff4, ring + so + SLOT);
      }
      ST(2);
      wg_barrier();
      bf16x8 qf[4];
#pragma unroll
      for (int t = 0; t < 4; ++t) qf[t] = *(const LAS bf16x8*)(lds + Q_OFF + q4 * 256 + g * 64 + t * 16);
      const unsigned tag = __builtin_amdgcn_readfirstlane(*(const LAS unsigned*)(lds + TAG_OFF));
      constexpr float C2 = 0.088388347648318440f * 1.4426950408889634f;
      f32x4 acc[8];
#pragma unroll
      for (int e = 0; e < 8; ++e) acc[e] = f32x4{0.f, 0.f, 0.f, 0.f};
      float m = -INFINITY, l = 0.f;
      auto scores = [&](const bf16x8 (&kf)[8]) -> bf16x8 {
        f32x4 s0 = {0.f, 0.f, 0.f, 0.f}, s1 = {0.f, 0.f, 0.f, 0.f};
#pragma unroll
        for (int t = 0; t < 4; ++t) {
          s0 = __builtin_amdgcn_mfma_f32_16x16x32_bf16(kf[t], qf[t], s0, 0, 0, 0);
          s1 = __builtin_amdgcn_mfma_f32_16x16x32_bf16(kf[4 + t], qf[t], s1, 0, 0, 0);
        }
        float s[8];
#pragma unroll
        for (int j = 0; j < 4; ++j) { s[j] = s0[j] * C2; s[4 + j] = s1[j] * C2; }
        float pm = fmaxf(fmaxf(fmaxf(s[0], s[1]), fmaxf(s[2], s[3])), fmaxf(fmaxf(s[4], s[5]), fmaxf(s[6], s[7])));
        pm = xrow_max(pm);
        if (__any(pm > m)) {
          const float mn = fmaxf(m, pm);
          const float alpha = __builtin_amdgcn_exp2f(m - mn);
          m = mn; l *= alpha;
          const float a0 = dpp<0x00>(alpha), a1 = dpp<0x55>(alpha), a2 = dpp<0xAA>(alpha), a3 = dpp<0xFF>(alpha);
#pragma unroll
          for (int e = 0; e < 8; ++e) { acc[e][0] *= a0; acc[e][1] *= a1; acc[e][2] *= a2; acc[e][3] *= a3; }
        }
        float p[8];
#pragma unroll
        for (int j = 0; j < 8; ++j) p[j] = __builtin_amdgcn_exp2f(s[j] - m);
        l += ((p[0] + p[1]) + (p[2] + p[3])) + ((p[4] + p[5]) + (p[6] + p[7]));
        const v4u pk = {cvtpk(p[0], p[1]), cvtpk(p[2], p[3]), cvtpk(p[4], p[5]), cvtpk(p[6], p[7])};
        return __builtin_bit_cast(bf16x8, pk);
      };
      auto pv = [&](const bf16x8 pf, const bf16x8 (&vf)[8]) {
#pragma unroll
        for (int e = 0; e < 8; ++e) acc[e] = __builtin_amdgcn_mfma_f32_16x16x32_bf16(pf, vf[e], acc[e], 0, 0, 0);
      };
#pragma unroll
      for (int u = 0; u < NREG; ++u) pv(scores(kr[u]), vr[u]);
#pragma unroll 1
      for (int u = NREG; u < nu; ++u) {
        const int so = (u & 1) * 2 * SLOT;
        const bool more = u + 2 < nu;
        if (more) wait_vm<INFL>(); else if (u + 1 < nu) wait_vm<24>(); else wait_vm<8>();
        bf16x8 kf[8];
        readK(so, kf);
        if (more) fill8<KPAD>(kb + uoff(u + 2), voff, voff4, ring + so);
        const bf16x8 pf = scores(kf);
        if (more) wait_vm<INFL>(); else if (u + 1 < nu) wait_vm<16>(); else wait_vm<0>();
        bf16x8 vf[8];
        readV(so + SLOT, vf);
        if (more) fill8<VPAD>(vb + uoff(u + 2), voff, voff4, ring + so + SLOT);
        pv(pf, vf);
      }
      ST(3);
      l = xrow_sum(l);
      LAS float* mrg = (LAS float*)(lds + MRG_OFF);
#pragma unroll
      for (int e = 0; e < 8; ++e) if ((e >> 1) == g) {
#pragma unroll
        for (int h = 0; h < 4; ++h) mrg[(wave * 4 + h) * MRG_REC + 4 + 16 * e + n] = acc[e][h];
      }
      if (lane < 4) { mrg[(wave * 4 + lane) * MRG_REC] = m; mrg[(wave * 4 + lane) * MRG_REC + 1] = l; }
      wg_barrier();
      const int d0 = 2 * lane;
      float M = -INFINITY;
#pragma unroll
      for (int w = 0; w < 4; ++w) M = fmaxf(M, mrg[(w * 4 + wave) * MRG_REC]);
      float L = 0.f, o0 = 0.f, o1 = 0.f;
#pragma unroll
      for (int w = 0; w < 4; ++w) {
        const LAS float* rec = mrg + (w * 4 + wave) * MRG_REC;
        const float wt = __builtin_amdgcn_exp2f(rec[0] - M);
        L = fmaf(wt, rec[1], L); o0 = fmaf(wt, rec[4 + d0], o0); o1 = fmaf(wt, rec[4 + d0 + 1], o1);
      }
      gu64* mine = (gu64*)(a.ws + WS_PG) + (size_t)(((kvh * 4 + wave) * 8 + b) * NSP + sp) * PG_REC;
      put(mine + lane, tag, __float_as_uint(o0));
      put(mine + 64 + lane, tag, __float_as_uint(o1));
      if (lane < 2) put(mine + 128 + lane, tag, __float_as_uint(lane ? L : M));
      ST(4); ST(5); ST(6); ST(7);
      ST_FLUSH();
    } else {
      __builtin_amdgcn_s_setprio(3);
      const unsigned tag = __builtin_amdgcn_readfirstlane(__hip_atomic_load((const gu32*)(a.ws + WS_E), RLX_AGENT)) + 1u;
      if (lane == 0) *(LAS unsigned*)(lds + TAG_OFF) = tag;
      ST(1);
      unsigned qv[4];
      sweep<4>((const gu64*)(a.ws + WS_QG) + (size_t)(kvh * 8 + b) * 256 + lane, 64, tag, qv);
#pragma unroll
      for (int k = 0; k < 4; ++k) *(LAS unsigned*)(lds + Q_OFF + (lane + 64 * k) * 4) = qv[k];
      ST(2);
      wg_barrier();
      ST(3); ST(4); ST(5); ST(6); ST(7);
      ST_FLUSH();
    }
    return;
  }
  if (wave >= 4) { ST_FLUSH(); return; }
  const int j = idx;
  const int prow = lane >> 3;
  const int ce = (lane & 7) ^ (2 * ((lane >> 4) & 1)), co = ce ^ 4;
  const int fsw = ((r4 >> 1) & 1) | ((g & 1) << 1);
  const int trow = (8 * g + r4) * 128 + q4 * 8;
  auto tfrag = [&](LAS unsigned char* tile, int s, int t) { LAS unsigned char* p = tile + s * 4096 + trow + ((t ^ fsw) << 5); return trfrag(p, p + 512); };
  bf16x8 xf[4]; unsigned ev;
  {
    const unsigned xo = (unsigned)((n & 7) * 1024 + wave * 256 + g * 16), zero = 0;
    asm volatile("global_load_dword %4, %7, %8 sc1\n\tglobal_load_dwordx4 %0, %5, %6\n\tglobal_load_dwordx4 %1, %5, %6 offset:64\n\tglobal_load_dwordx4 %2, %5, %6 offset:128\n\tglobal_load_dwordx4 %3, %5, %6 offset:192"
                 : "=&v"(xf[0]), "=&v"(xf[1]), "=&v"(xf[2]), "=&v"(xf[3]), "=&v"(ev) : "v"(xo), "s"(a.x), "v"(zero), "s"(a.ws) : "memory");
  }
  {
    const unsigned char* src = a.wq + (size_t)(xg * 512 + j * 64) * 2;
    const unsigned ve = (unsigned)((wave * 128 + prow) * 8192 + ce * 16), vo = (unsigned)((wave * 128 + 8 + prow) * 8192 + co * 16);
#pragma unroll
    for (int i = 0; i < 8; ++i) { wpiece(src, ve + i * 16 * 8192, ldsb + T0_OFF + wave * 16384 + i * 2048); wpiece(src, vo + i * 16 * 8192, ldsb + T0_OFF + wave * 16384 + i * 2048 + 1024); }
  }
  asm volatile("s_waitcnt vmcnt(0)" : "+v"(xf[0]), "+v"(xf[1]), "+v"(xf[2]), "+v"(xf[3]), "+v"(ev) :: "memory");
  const unsigned tag = __builtin_amdgcn_readfirstlane(ev) + 1u;
  ST(1);
  auto proj = [&](LAS unsigned char* tile, LAS unsigned char* dst) {
    f32x4 c[4];
#pragma unroll
    for (int t = 0; t < 4; ++t) c[t] = f32x4{0.f, 0.f, 0.f, 0.f};
#pragma unroll
    for (int s = 0; s < 4; ++s)
#pragma unroll
      for (int t = 0; t < 4; ++t) c[t] = __builtin_amdgcn_mfma_f32_16x16x32_bf16(tfrag(tile, s, t), xf[s], c[t], 0, 0, 0);
#pragma unroll
    for (int t = 0; t < 4; ++t) *(LAS f32x4*)(dst + t * 1024 + lane * 16) = c[t];
  };
  proj(lds + T0_OFF + wave * 16384, lds + P_OFF + wave * 4096);
  wg_barrier();
  {
    const LAS f32x4* qp = (const LAS f32x4*)(lds + P_OFF + wave * 1024) + lane;
    const f32x4 c0 = (qp[0] + qp[256]) + (qp[512] + qp[768]);
    const unsigned val = n < 8 ? cvtpk(c0[0], c0[1]) : cvtpk(c0[2], c0[3]);
    put((gu64*)(a.ws + WS_QG) + (size_t)(xg * 8 + (n & 7)) * 256 + j * 32 + wave * 8 + g * 2 + (n >> 3), tag, val);
  }
  ST(2);
  {
    const int jc = j < 7 ? j : 6;
    const size_t col = (size_t)(xg * 224 + jc * 32) * 2;
    const unsigned char* se = (ce < 4 ? a.wg : a.wu) + (size_t)(wave * 128 + prow) * (FF * 2) + col + (ce & 3) * 16;
    const unsigned char* so = (co < 4 ? a.wg : a.wu) + (size_t)(wave * 128 + 8 + prow) * (FF * 2) + col + (co & 3) * 16;
#pragma unroll
    for (int i = 0; i < 8; ++i) { wpiece64(se + (size_t)i * 16 * FF * 2, ldsb + T1_OFF + wave * 16384 + i * 2048); wpiece64(so + (size_t)i * 16 * FF * 2, ldsb + T1_OFF + wave * 16384 + i * 2048 + 1024); }
  }
  wait_vm<0>();
  const int head = xg * 4 + (j >> 1);
  {
    const unsigned char* src = a.wo + ((size_t)(head * 128) * DM + (j & 1) * 256 + wave * 64) * 2;
    const unsigned ve = (unsigned)(prow * 1024 + ce * 16), vo = (unsigned)((8 + prow) * 1024 + co * 16);
#pragma unroll
    for (int i = 0; i < 8; ++i) { wpiece(src, ve + i * 16 * 1024, ldsb + T0_OFF + wave * 16384 + i * 2048); wpiece(src, vo + i * 16 * 1024, ldsb + T0_OFF + wave * 16384 + i * 2048 + 1024); }
  }
  proj(lds + T1_OFF + wave * 16384, lds + T1_OFF + wave * 16384);
  wg_barrier();
  {
    const unsigned char* src = a.wd + ((size_t)(xg * 224) * DM + j * 64) * 2;
#pragma unroll
    for (int i = 0; i < 7; ++i) { const int pc = wave * 7 + i; wpiece(src, (unsigned)((pc * 8 + prow) * 1024 + ((pc & 1) ? co : ce) * 16), ldsb + WD_OFF + pc * 1024); }
  }
  if (j < 7 && wave < 2) {
    const LAS f32x4* gp = (const LAS f32x4*)(lds + T1_OFF + wave * 1024) + lane;
    const f32x4 cg = (gp[0] + gp[1024]) + (gp[2048] + gp[3072]);
    const f32x4 cu = (gp[128] + gp[1024 + 128]) + (gp[2048 + 128] + gp[3072 + 128]);
    float hv[4];
#pragma unroll
    for (int k = 0; k < 4; ++k) hv[k] = rbf(sigmoidf_(rbf(cg[k]))) * rbf(cu[k]);
    const unsigned val = n < 8 ? cvtpk(hv[0], hv[1]) : cvtpk(hv[2], hv[3]);
    put((gu64*)(a.ws + WS_HG) + (size_t)(xg * 8 + (n & 7)) * 112 + j * 16 + wave * 8 + g * 2 + (n >> 3), tag, val);
  }
  float gate;
  {
    const v4u xg8 = *(const GAS v4u*)(a.x + (size_t)j * 1024 + lane * 16), gw8 = *(const GAS v4u*)(a.gw + lane * 16);
    float s = 0.f;
#pragma unroll
    for (int k = 0; k < 4; ++k) {
      s += rbf(bf2f(xg8[k] & 0xffffu) * bf2f(gw8[k] & 0xffffu));
      s += rbf(bf2f(xg8[k] >> 16) * bf2f(gw8[k] >> 16));
    }
    s += dpp<0xB1>(s); s += dpp<0x4E>(s); s += dpp<0x141>(s); s += dpp<0x128>(s);
    s = xrow_sum(s);
    gate = rbf(sigmoidf_(rbf(s)));
  }
  wait_vm<0>();
  wg_barrier();
  bf16x8 wf[16];
#pragma unroll
  for (int s = 0; s < 4; ++s)
#pragma unroll
    for (int t = 0; t < 4; ++t) wf[s * 4 + t] = tfrag(lds + T0_OFF + wave * 16384, s, t);
  {
    unsigned hv[14];
    const gu64* hg = (const gu64*)(a.ws + WS_HG) + (size_t)(xg * 8 + (n & 7)) * 112 + g * 4 + (n >> 3) * 2;
    const u64 t0 = rt();
    for (;;) {
      bool ok = true;
#pragma unroll
      for (int s = 0; s < 7; ++s) {
        const u64 y0 = __hip_atomic_load(hg + s * 16, RLX_AGENT), y1 = __hip_atomic_load(hg + s * 16 + 1, RLX_AGENT);
        hv[2 * s] = (unsigned)y0; hv[2 * s + 1] = (unsigned)y1; ok &= (unsigned)(y0 >> 32) == tag && (unsigned)(y1 >> 32) == tag;
      }
      if (__all(ok)) break;
      if (rt() - t0 > SPIN_CAP) break;
      __builtin_amdgcn_s_sleep(8);
    }
    f32x4 c0 = {0.f, 0.f, 0.f, 0.f};
#pragma unroll
    for (int s = 0; s < 7; ++s) {
      const unsigned o0 = (unsigned)__builtin_amdgcn_mov_dpp((int)hv[2 * s], 0x128, 0xf, 0xf, true), o1 = (unsigned)__builtin_amdgcn_mov_dpp((int)hv[2 * s + 1], 0x128, 0xf, 0xf, true);
      const v4u hf = n < 8 ? v4u{hv[2 * s], hv[2 * s + 1], o0, o1} : v4u{o0, o1, hv[2 * s], hv[2 * s + 1]};
      c0 = __builtin_amdgcn_mfma_f32_16x16x32_bf16(__builtin_bit_cast(bf16x8, hf), tfrag(lds + WD_OFF, s, wave), c0, 0, 0, 0);
    }
    gu64* dg = (gu64*)(a.ws + WS_DG) + (size_t)(((g & 1) * 4 + (g >> 1) * 2) * 32 + j * 4 + wave) * 128 + xg * 16 + n;
    put(dg, tag, __float_as_uint(g < 2 ? c0[0] : c0[2]));
    put(dg + 32 * 128, tag, __float_as_uint(g < 2 ? c0[1] : c0[3]));
  }
  ST(3);
  const int c16 = xg * 4 + wave;
  float bm;
  {
    unsigned dv[2];
    sweep<2>((const gu64*)(a.ws + WS_DG) + (size_t)(j * 32 + c16) * 128 + lane, 64, tag, dv);
    bm = rbf(xrow_sum(__uint_as_float(dv[0]) + __uint_as_float(dv[1])));
  }
  ST(4);
  {
    const gu64* grp = (const gu64*)(a.ws + WS_PG) + (size_t)((head * 8 + wave * 2) * NSP) * PG_REC;
    const u64 t0 = rt();
    for (;;) {
      const u64 y = __hip_atomic_load(grp + (size_t)(lane < 6 ? lane : 0) * PG_REC + 128, RLX_AGENT);
      if (__any((unsigned)(y >> 32) == tag)) break;
      if (rt() - t0 > SPIN_CAP) break;
      __builtin_amdgcn_s_sleep(SLEEP0);
    }
    float pm_[6], pl_[6], p0_[6], p1_[6];
    unsigned missing = 63u;
    for (;;) {
      u64 x0[6], x1[6], xm[6], xl[6];
#pragma unroll
      for (int s = 0; s < 6; ++s) if (missing & (1u << s)) {
        const gu64* r = grp + (size_t)s * PG_REC;
        x0[s] = __hip_atomic_load(r + lane, RLX_AGENT); x1[s] = __hip_atomic_load(r + 64 + lane, RLX_AGENT);
        xm[s] = __hip_atomic_load(r + 128, RLX_AGENT); xl[s] = __hip_atomic_load(r + 129, RLX_AGENT);
      }
#pragma unroll
      for (int s = 0; s < 6; ++s) if (missing & (1u << s)) {
        const bool ok = (unsigned)(x0[s] >> 32) == tag && (unsigned)(x1[s] >> 32) == tag && (unsigned)(xm[s] >> 32) == tag && (unsigned)(xl[s] >> 32) == tag;
        if (__all(ok)) {
          p0_[s] = __uint_as_float((unsigned)x0[s]); p1_[s] = __uint_as_float((unsigned)x1[s]); pm_[s] = __uint_as_float((unsigned)xm[s]); pl_[s] = __uint_as_float((unsigned)xl[s]);
          missing &= ~(1u << s);
        }
      }
      if (!missing) break;
      if (rt() - t0 > SPIN_CAP) break;
      __builtin_amdgcn_s_sleep(SLEEP1);
    }
    ST(5);
#pragma unroll
    for (int bb = 0; bb < 2; ++bb) {
      const float MM = fmaxf(fmaxf(pm_[3 * bb], pm_[3 * bb + 1]), pm_[3 * bb + 2]);
      float LL = 0.f, y0 = 0.f, y1 = 0.f;
#pragma unroll
      for (int s = 3 * bb; s < 3 * bb + 3; ++s) { const float wt = __builtin_amdgcn_exp2f(pm_[s] - MM); LL = fmaf(wt, pl_[s], LL); y0 = fmaf(wt, p0_[s], y0); y1 = fmaf(wt, p1_[s], y1); }
      const float inv = __builtin_amdgcn_rcpf(LL);
      *(LAS unsigned*)(lds + O_OFF + (wave * 2 + bb) * O_PITCH + lane * 4) = cvtpk(y0 * inv, y1 * inv);
    }
  }
  wg_barrier();
  {
    f32x4 c[4];
#pragma unroll
    for (int t = 0; t < 4; ++t) c[t] = f32x4{0.f, 0.f, 0.f, 0.f};
    bf16x8 of[4];
#pragma unroll
    for (int s = 0; s < 4; ++s) of[s] = *(const LAS bf16x8*)(lds + O_OFF + (n & 7) * O_PITCH + s * 64 + g * 16);
#pragma unroll
    for (int s = 0; s < 4; ++s)
#pragma unroll
      for (int t = 0; t < 4; ++t) c[t] = __builtin_amdgcn_mfma_f32_16x16x32_bf16(of[s], wf[s * 4 + t], c[t], 0, 0, 0);
    gu64* ag = (gu64*)(a.ws + WS_AG) + (size_t)(((g & 1) * 4 + (g >> 1) * 2) * 32 + (j & 1) * 16 + wave * 4) * 512 + head * 16 + n;
#pragma unroll
    for (int t = 0; t < 4; ++t) {
      put(ag + (size_t)t * 512, tag, __float_as_uint(g < 2 ? c[t][0] : c[t][2]));
      put(ag + (size_t)(32 + t) * 512, tag, __float_as_uint(g < 2 ? c[t][1] : c[t][3]));
    }
  }
  ST(6);
  {
    const gu64* ag = (const gu64*)(a.ws + WS_AG) + (size_t)(j * 32 + c16) * 512 + lane;
    unsigned av[8];
    unsigned missing = 255u;
    const u64 t0 = rt();
    for (;;) {
      u64 y[8];
#pragma unroll
      for (int k = 0; k < 8; ++k) if (missing & (1u << k)) y[k] = __hip_atomic_load(ag + k * 64, RLX_AGENT);
#pragma unroll
      for (int k = 0; k < 8; ++k) if (missing & (1u << k)) { if (__all((unsigned)(y[k] >> 32) == tag)) { av[k] = (unsigned)y[k]; missing &= ~(1u << k); } }
      if (!missing) break;
      if (rt() - t0 > SPIN_CAP) break;
      __builtin_amdgcn_s_sleep(SLEEP2);
    }
    float s = 0.f;
#pragma unroll
    for (int k = 0; k < 8; ++k) s += __uint_as_float(av[k]);
    const float A = rbf(xrow_sum(s));
    const float r = rbf(gate * A) + rbf(rbf(1.f - gate) * bm);
    if (lane < 16) a.out[j * DM + c16 * 16 + lane] = (u16)(cvtpk(r, r) & 0xffffu);
    if (bid == 0 && wave == 0 && lane == 0) __hip_atomic_store((gu32*)(a.ws + WS_E), tag, RLX_AGENT);
  }
  while (rt() - tstart_ < 8000) __builtin_amdgcn_s_sleep(1);
}

extern "C" void kernel_launch(void* const* d_in, const int* in_sizes, int n_in, void* d_out, int out_size, void* d_ws, size_t ws_size, hipStream_t stream) {
  Args a;
  a.x = (const unsigned char*)d_in[0]; a.K = (const unsigned char*)d_in[1]; a.V = (const unsigned char*)d_in[2];
  a.wq = (const unsigned char*)d_in[3]; a.wo = (const unsigned char*)d_in[4];
  a.wg = (const unsigned char*)d_in[6]; a.wu = (const unsigned char*)d_in[7]; a.wd = (const unsigned char*)d_in[8]; a.gw = (const unsigned char*)d_in[9];
  a.ws = (unsigned char*)d_ws; a.out = (u16*)d_out;
  (void)hipFuncSetAttribute((const void*)fused_kernel, hipFuncAttributeMaxDynamicSharedMemorySize, LDS_BYTES);
  fused_kernel<<<256, 320, LDS_BYTES, stream>>>(a);
}
```

```cpp
#define PROLE 0
#define PIDX 0
#define NM 20
#include <hip/hip_runtime.h>
#include <hip/hip_bf16.h>
#include <cstdint>

#define GAS __attribute__((address_space(1)))
#define LAS __attribute__((address_space(3)))
typedef unsigned v4u __attribute__((ext_vector_type(4)));
typedef unsigned v2u __attribute__((ext_vector_type(2)));
typedef float f32x4 __attribute__((ext_vector_type(4)));
typedef __bf16 bf16x8 __attribute__((ext_vector_type(8)));
typedef short s16x4 __attribute__((ext_vector_type(4)));
typedef short s16x8 __attribute__((ext_vector_type(8)));
typedef unsigned short u16;
typedef unsigned long long u64;
typedef GAS u64 gu64;
typedef GAS unsigned gu32;
#define RLX_AGENT __ATOMIC_RELAXED, __HIP_MEMORY_SCOPE_AGENT

constexpr int NB = 8, DM = 512, HQ = 32, HKV = 8, SK = 8192, DH = 128, FF = 1792;
#define W_AUX " nt"

__device__ __forceinline__ float bf2f(unsigned v) { return __builtin_bit_cast(float, v << 16); }
__device__ __forceinline__ unsigned cvtpk(float lo, float hi) { unsigned r; asm("v_cvt_pk_bf16_f32 %0, %1, %2" : "=v"(r) : "v"(lo), "v"(hi)); return r; }
__device__ __forceinline__ float rbf(float x) { return __builtin_bit_cast(float, cvtpk(x, x) << 16); }
template <int CTRL> __device__ __forceinline__ float dpp(float x) {
  return __builtin_bit_cast(float, __builtin_amdgcn_mov_dpp(__builtin_bit_cast(int, x), CTRL, 0xf, 0xf, true));
}
__device__ __forceinline__ float xrow_max(float x) {
  auto s = __builtin_amdgcn_permlane16_swap(__float_as_uint(x), __float_as_uint(x), false, false);
  x = fmaxf(__uint_as_float(s[0]), __uint_as_float(s[1]));
  auto t = __builtin_amdgcn_permlane32_swap(__float_as_uint(x), __float_as_uint(x), false, false);
  return fmaxf(__uint_as_float(t[0]), __uint_as_float(t[1]));
}
__device__ __forceinline__ float xrow_sum(float x) {
  auto s = __builtin_amdgcn_permlane16_swap(__float_as_uint(x), __float_as_uint(x), false, false);
  x = __uint_as_float(s[0]) + __uint_as_float(s[1]);
  auto t = __builtin_amdgcn_permlane32_swap(__float_as_uint(x), __float_as_uint(x), false, false);
  return __uint_as_float(t[0]) + __uint_as_float(t[1]);
}
template <int N> __device__ __forceinline__ void wait_vm() { asm volatile("s_waitcnt vmcnt(%0)" ::"n"(N) : "memory"); }
__device__ __forceinline__ void wg_barrier() { asm volatile("s_waitcnt lgkmcnt(0)\n\ts_barrier" ::: "memory"); }
__device__ __forceinline__ u64 rt() { return __builtin_amdgcn_s_memrealtime(); }
__device__ __forceinline__ float sigmoidf_(float v) { return 1.f / (1.f + __expf(-v)); }

#define FILL8_BODY(KV_AUX) asm volatile( \
      "s_mov_b32 %0, m0\n\t" \
      "s_mov_b32 m0, %4\n\ts_nop 0\n\t" \
      "global_load_lds_dwordx4 %1, %3" KV_AUX "\n\t" \
      "s_add_u32 m0, m0, %5\n\ts_nop 0\n\t" \
      "global_load_lds_dwordx4 %1, %3 offset:1024" KV_AUX "\n\t" \
      "s_add_u32 m0, m0, %5\n\ts_nop 0\n\t" \
      "global_load_lds_dwordx4 %1, %3 offset:2048" KV_AUX "\n\t" \
      "s_add_u32 m0, m0, %5\n\ts_nop 0\n\t" \
      "global_load_lds_dwordx4 %1, %3 offset:3072" KV_AUX "\n\t" \
      "s_add_u32 m0, m0, %6\n\ts_nop 0\n\t" \
      "global_load_lds_dwordx4 %2, %3" KV_AUX "\n\t" \
      "s_add_u32 m0, m0, %5\n\ts_nop 0\n\t" \
      "global_load_lds_dwordx4 %2, %3 offset:1024" KV_AUX "\n\t" \
      "s_add_u32 m0, m0, %5\n\ts_nop 0\n\t" \
      "global_load_lds_dwordx4 %2, %3 offset:2048" KV_AUX "\n\t" \
      "s_add_u32 m0, m0, %5\n\ts_nop 0\n\t" \
      "global_load_lds_dwordx4 %2, %3 offset:3072" KV_AUX "\n\t" \
      "s_mov_b32 m0, %0" \
      : "=&s"(keep) \
      : "v"(voff), "v"(voff4), "s"(sbase), "s"(dst), "n"(PAD), "n"(4096 + PAD) \
      : "memory", "scc")
template <int PAD, int NT> __device__ __forceinline__ void fill8p(const void* sbase, unsigned voff, unsigned voff4, unsigned dst) {
  unsigned keep;
  if constexpr (NT) FILL8_BODY(" nt"); else FILL8_BODY("");
}
#define K_NT 1
#define V_NT 1
template <int PAD> __device__ __forceinline__ void fill8(const void* sbase, unsigned voff, unsigned voff4, unsigned dst) {
  fill8p<PAD, (PAD == 16) ? K_NT : V_NT>(sbase, voff, voff4, dst);
}
__device__ __forceinline__ void wpiece(const void* sbase, unsigned voff, unsigned dst) {
  unsigned keep;
  asm volatile("s_mov_b32 %0, m0\n\ts_mov_b32 m0, %3\n\ts_nop 0\n\tglobal_load_lds_dwordx4 %1, %2" W_AUX "\n\ts_mov_b32 m0, %0"
               : "=&s"(keep) : "v"(voff), "s"(sbase), "s"(dst) : "memory");
}
__device__ __forceinline__ void wpiece64(const void* vaddr, unsigned dst) {
  unsigned keep;
  asm volatile("s_mov_b32 %0, m0\n\ts_mov_b32 m0, %2\n\ts_nop 0\n\tglobal_load_lds_dwordx4 %1, off" W_AUX "\n\ts_mov_b32 m0, %0"
               : "=&s"(keep) : "v"(vaddr), "s"(dst) : "memory");
}
__device__ __forceinline__ bf16x8 trfrag(LAS unsigned char* p0, LAS unsigned char* p1) {
  const s16x4 a = __builtin_amdgcn_ds_read_tr16_b64_v4i16((LAS s16x4*)p0), b = __builtin_amdgcn_ds_read_tr16_b64_v4i16((LAS s16x4*)p1);
  return __builtin_bit_cast(bf16x8, __builtin_shufflevector(a, b, 0, 1, 2, 3, 4, 5, 6, 7));
}
__device__ __forceinline__ void put(gu64* p, unsigned tag, unsigned val) { __hip_atomic_store(p, ((u64)tag << 32) | val, RLX_AGENT); }
#define INFL 24
#define SLEEP0 16
#define SLEEP1 1
#define SLEEP2 1
#define MLP_DELAY 100
constexpr u64 SPIN_CAP = 5000000;
template <int N> __device__ __forceinline__ void sweep(const gu64* g, int stride, unsigned tag, unsigned (&v)[N]) {
  const u64 t0 = rt();
  for (;;) {
    bool ok = true;
#pragma unroll
    for (int k = 0; k < N; ++k) { const u64 x = __hip_atomic_load(g + k * stride, RLX_AGENT); v[k] = (unsigned)x; ok &= (unsigned)(x >> 32) == tag; }
    if (__all(ok)) return;
    if (rt() - t0 > SPIN_CAP) return;
    __builtin_amdgcn_s_sleep(1);
  }
}

typedef __amdgpu_buffer_rsrc_t rsrc_t;
__device__ __forceinline__ v4u ld2g(rsrc_t r, unsigned voff, unsigned soff) { return __builtin_amdgcn_raw_buffer_load_b128(r, voff, soff, 16); }
__device__ __forceinline__ void st2g(rsrc_t r, unsigned voff, unsigned tag, float v0, float v1) {
  __builtin_amdgcn_raw_buffer_store_b128(v4u{__float_as_uint(v0), tag, __float_as_uint(v1), tag}, r, voff, 0, 16);
}
constexpr unsigned OOB = 0x01000000u;
constexpr unsigned SPIN_ITERS = 1u << 17;
constexpr int KPAD = 16, VPAD = 32, KBLK = 1024 + KPAD, VBLK = 1024 + VPAD, SLOT = 8 * VBLK;
constexpr int RING = 4 * SLOT;
constexpr int MRG_OFF = 4 * RING, MRG_REC = 132;
constexpr int QP_OFF = MRG_OFF + 4 * 4 * MRG_REC * 4, GP_OFF = QP_OFF + 8192;
constexpr int O_OFF = GP_OFF + 8192, O_PITCH = 272;
constexpr int Q_OFF = O_OFF + 8 * O_PITCH;
constexpr int TAG_OFF = Q_OFF + 1024;
constexpr int T0_OFF = 0, T1_OFF = 65536;
constexpr int LDS_BYTES = TAG_OFF + 16;
static_assert(LDS_BYTES <= 163840, "lds");

constexpr int NA = (256 - 12 * NM) / 4;
static_assert(12 * NM + 4 * NA == 256 && NA >= 2, "unit deal");
constexpr int NSP = 4, PG_REC = 144;
constexpr size_t WS_E = 0;
constexpr size_t WS_QG = 4096;
constexpr size_t WS_HG = WS_QG + 8 * 8 * 256 * 8;
constexpr size_t WS_DG = WS_HG + 65536;
constexpr size_t WS_PG = WS_DG + 8 * 32 * 128 * 8;
constexpr size_t WS_AG = WS_PG + (size_t)32 * 8 * NSP * PG_REC * 8;
constexpr size_t WS_END = WS_AG + (size_t)8 * 32 * 512 * 8;
constexpr size_t WS_ST = (size_t)64 << 20;

struct Args { const unsigned char *x, *K, *V, *wq, *wo, *wg, *wu, *wd, *gw; unsigned char* ws; u16* out; };

#define STAMP 1
constexpr int NSTAMP = 8;
#define ST(i) do { stamp[i] = rt(); } while (0)
#define ST_FLUSH() do { if (lane == 0) { _Pragma("unroll") for (int i_ = 0; i_ < NSTAMP; ++i_) ((gu64*)(a.ws + WS_ST))[(blockIdx.x * 5 + wave) * 16 + i_] = stamp[i_]; } } while (0)
#undef ST_FLUSH
#define ST_FLUSH() do { if ((wave == 4 ? 2 : (blockIdx.x < 64 ? 1 : 0)) == PROLE && (PROLE != 2 || blockIdx.x >= 64)) { while (rt() - stamp[PIDX] < 8000) __builtin_amdgcn_s_sleep(1); } } while (0)
#define ST_ARG , u64 (&stamp)[NSTAMP]
#define ST_PASS , stamp
#define NREG 3
#define PINFL 16

template <int NU, int USTRIDE, int PRE>
__device__ __forceinline__ void stream_split(const Args& a, LAS unsigned char* lds, rsrc_t wsr, int wave, int lane, int b, int kvh, int sp, int u0 ST_ARG) {
  const int n = lane & 15, g = lane >> 4, q4 = n & 3, r4 = n >> 2;
  const unsigned ring = (unsigned)(size_t)lds + wave * RING;
  LAS unsigned char* lring = lds + wave * RING;
  const int pair = b * HKV + kvh;
  const int rot = (pair * 5) % NU;
  const unsigned char* kb = a.K + (size_t)pair * (SK * 256) + (size_t)u0 * 8192;
  const unsigned char* vb = a.V + (size_t)pair * (SK * 256) + (size_t)u0 * 8192;
  const unsigned voff = (unsigned)(g * 256 + (n ^ (g << 2)) * 16), voff4 = voff + 4096;
  auto uoff = [&](int u) { int t = u + rot; if (t >= NU) t -= NU; return (size_t)(t * USTRIDE) * 8192; };
  const int kbase = r4 * KBLK + q4 * 256 + ((g ^ q4) * 64);
  int vbase[4];
#pragma unroll
  for (int e2 = 0; e2 < 4; ++e2) vbase[e2] = g * VBLK + r4 * 256 + ((e2 ^ r4) * 64) + (q4 >> 1) * 16 + (q4 & 1) * 8;
  auto readK = [&](int so, bf16x8 (&kf)[8]) {
#pragma unroll
    for (int i = 0; i < 8; ++i) kf[i] = *(const LAS bf16x8*)(lring + so + kbase + (i >> 2) * 4 * KBLK + (i & 3) * 16);
    asm volatile("s_waitcnt lgkmcnt(0)" : "+v"(kf[0]), "+v"(kf[1]), "+v"(kf[2]), "+v"(kf[3]), "+v"(kf[4]), "+v"(kf[5]), "+v"(kf[6]), "+v"(kf[7]) :: "memory");
  };
  auto readV = [&](int so, bf16x8 (&vf)[8]) {
#pragma unroll
    for (int e = 0; e < 8; ++e) { LAS unsigned char* p0 = lring + so + vbase[e >> 1] + (e & 1) * 32; vf[e] = trfrag(p0, p0 + 4 * VBLK); }
    asm volatile("s_waitcnt lgkmcnt(0)" : "+v"(vf[0]), "+v"(vf[1]), "+v"(vf[2]), "+v"(vf[3]), "+v"(vf[4]), "+v"(vf[5]), "+v"(vf[6]), "+v"(vf[7]) :: "memory");
  };
  fill8<KPAD>(kb + uoff(0), voff, voff4, ring);
  fill8<VPAD>(vb + uoff(0), voff, voff4, ring + SLOT);
  if (PINFL < 16) wait_vm<PINFL>();
  fill8<KPAD>(kb + uoff(1), voff, voff4, ring + 2 * SLOT);
  if (PINFL < 24) wait_vm<PINFL>();
  fill8<VPAD>(vb + uoff(1), voff, voff4, ring + 3 * SLOT);
  bf16x8 kr[PRE ? PRE : 1][8], vr[PRE ? PRE : 1][8];
  if constexpr (PRE > 0) {
    ST(1);
#pragma unroll
    for (int u = 0; u < PRE; ++u) {
      const int so = (u & 1) * 2 * SLOT;
      wait_vm<PINFL>();
      readK(so, kr[u]);
      fill8<KPAD>(kb + uoff(u + 2), voff, voff4, ring + so);
      wait_vm<PINFL>();
      readV(so + SLOT, vr[u]);
      fill8<VPAD>(vb + uoff(u + 2), voff, voff4, ring + so + SLOT);
    }
    ST(2);
    wg_barrier();
  }
  bf16x8 qf[4];
#pragma unroll
  for (int t = 0; t < 4; ++t) qf[t] = *(const LAS bf16x8*)(lds + Q_OFF + q4 * 256 + g * 64 + t * 16);
  const unsigned tag = __builtin_amdgcn_readfirstlane(*(const LAS unsigned*)(lds + TAG_OFF));
  constexpr float C2 = 0.088388347648318440f * 1.4426950408889634f;
  f32x4 acc[8];
#pragma unroll
  for (int e = 0; e < 8; ++e) acc[e] = f32x4{0.f, 0.f, 0.f, 0.f};
  float m = -INFINITY, l = 0.f;
  auto scores = [&](const bf16x8 (&kf)[8]) -> bf16x8 {
    f32x4 s0 = {0.f, 0.f, 0.f, 0.f}, s1 = {0.f, 0.f, 0.f, 0.f};
#pragma unroll
    for (int t = 0; t < 4; ++t) {
      s0 = __builtin_amdgcn_mfma_f32_16x16x32_bf16(kf[t], qf[t], s0, 0, 0, 0);
      s1 = __builtin_amdgcn_mfma_f32_16x16x32_bf16(kf[4 + t], qf[t], s1, 0, 0, 0);
    }
    float s[8];
#pragma unroll
    for (int j = 0; j < 4; ++j) { s[j] = s0[j] * C2; s[4 + j] = s1[j] * C2; }
    float pm = fmaxf(fmaxf(fmaxf(s[0], s[1]), fmaxf(s[2], s[3])), fmaxf(fmaxf(s[4], s[5]), fmaxf(s[6], s[7])));
    pm = xrow_max(pm);
    if (__any(pm > m)) {
      const float mn = fmaxf(m, pm);
      const float alpha = __builtin_amdgcn_exp2f(m - mn);
      m = mn; l *= alpha;
      const float a0 = dpp<0x00>(alpha), a1 = dpp<0x55>(alpha), a2 = dpp<0xAA>(alpha), a3 = dpp<0xFF>(alpha);
#pragma unroll
      for (int e = 0; e < 8; ++e) { acc[e][0] *= a0; acc[e][1] *= a1; acc[e][2] *= a2; acc[e][3] *= a3; }
    }
    float p[8];
#pragma unroll
    for (int j = 0; j < 8; ++j) p[j] = __builtin_amdgcn_exp2f(s[j] - m);
    l += ((p[0] + p[1]) + (p[2] + p[3])) + ((p[4] + p[5]) + (p[6] + p[7]));
    const v4u pk = {cvtpk(p[0], p[1]), cvtpk(p[2], p[3]), cvtpk(p[4], p[5]), cvtpk(p[6], p[7])};
    return __builtin_bit_cast(bf16x8, pk);
  };
  auto pv = [&](const bf16x8 pf, const bf16x8 (&vf)[8]) {
#pragma unroll
    for (int e = 0; e < 8; ++e) acc[e] = __builtin_amdgcn_mfma_f32_16x16x32_bf16(pf, vf[e], acc[e], 0, 0, 0);
  };
#pragma unroll
  for (int u = 0; u < PRE; ++u) pv(scores(kr[u]), vr[u]);
#pragma unroll 1
  for (int u = PRE; u < NU; ++u) {
    const int so = (u & 1) * 2 * SLOT;
    const bool more = u + 2 < NU;
    if (more) wait_vm<INFL>(); else if (u + 1 < NU) wait_vm<24>(); else wait_vm<8>();
    bf16x8 kf[8];
    readK(so, kf);
    if (more) fill8<KPAD>(kb + uoff(u + 2), voff, voff4, ring + so);
    const bf16x8 pf = scores(kf);
    if (more) wait_vm<INFL>(); else if (u + 1 < NU) wait_vm<16>(); else wait_vm<0>();
    bf16x8 vf[8];
    readV(so + SLOT, vf);
    if (more) fill8<VPAD>(vb + uoff(u + 2), voff, voff4, ring + so + SLOT);
    pv(pf, vf);
  }
  ST(3);
  l = xrow_sum(l);
  LAS float* mrg = (LAS float*)(lds + MRG_OFF);
#pragma unroll
  for (int e = 0; e < 8; ++e) if ((e >> 1) == g) {
#pragma unroll
    for (int h = 0; h < 4; ++h) mrg[(wave * 4 + h) * MRG_REC + 4 + 16 * e + n] = acc[e][h];
  }
  if (lane < 4) { mrg[(wave * 4 + lane) * MRG_REC] = m; mrg[(wave * 4 + lane) * MRG_REC + 1] = l; }
  wg_barrier();
  const int d0 = 2 * lane;
  float M = -INFINITY;
#pragma unroll
  for (int w = 0; w < 4; ++w) M = fmaxf(M, mrg[(w * 4 + wave) * MRG_REC]);
  float L = 0.f, o0 = 0.f, o1 = 0.f;
#pragma unroll
  for (int w = 0; w < 4; ++w) {
    const LAS float* rec = mrg + (w * 4 + wave) * MRG_REC;
    const float wt = __builtin_amdgcn_exp2f(rec[0] - M);
    L = fmaf(wt, rec[1], L); o0 = fmaf(wt, rec[4 + d0], o0); o1 = fmaf(wt, rec[4 + d0 + 1], o1);
  }
  const unsigned mine = (unsigned)WS_PG + (unsigned)(((kvh * 4 + wave) * 8 + b) * NSP + sp) * (PG_REC * 8);
  st2g(wsr, mine + lane * 16, tag, o0, o1);
  if (lane == 0) st2g(wsr, mine + 1024, tag, M, L);
}

__global__ void __launch_bounds__(320, 1) fused_kernel(Args a) {
  extern __shared__ __attribute__((aligned(16))) unsigned char lds_[];
  LAS unsigned char* lds = (LAS unsigned char*)lds_;
  const unsigned ldsb = (unsigned)(size_t)lds;
  const int tid = threadIdx.x, lane = tid & 63, wave = __builtin_amdgcn_readfirstlane(tid >> 6);
  const int bid = blockIdx.x, xg = bid & 7, idx = bid >> 3;
  const int n = lane & 15, g = lane >> 4, q4 = n & 3, r4 = n >> 2;
  const rsrc_t wsr = __builtin_amdgcn_make_buffer_rsrc(a.ws, 0, (int)WS_END, 0x00020000);
  u64 stamp[NSTAMP]; for (int i = 0; i < NSTAMP; ++i) stamp[i] = rt();
  const bool aux = idx < 8;
  if (wave == 4) {
    __builtin_amdgcn_s_setprio(3);
    const int b = aux ? idx : (idx - 8) / 3;
    const unsigned tag = __builtin_amdgcn_readfirstlane(__hip_atomic_load((const gu32*)(a.ws + WS_E), RLX_AGENT)) + 1u;
    if (lane == 0) *(LAS unsigned*)(lds + TAG_OFF) = tag;
    if (aux) wg_barrier();
    ST(1);
    unsigned qv[4];
    sweep<4>((const gu64*)(a.ws + WS_QG) + (size_t)(xg * 8 + b) * 256 + lane, 64, tag, qv);
#pragma unroll
    for (int k = 0; k < 4; ++k) *(LAS unsigned*)(lds + Q_OFF + (lane + 64 * k) * 4) = qv[k];
    ST(2);
    wg_barrier();
    ST(3); ST(4); ST(5); ST(6); ST(7);
    ST_FLUSH();
    __builtin_amdgcn_endpgm();
  }
  if (!aux) {
    const int b = (idx - 8) / 3, sp = (idx - 8) % 3;
    stream_split<NM, 12, NREG>(a, lds, wsr, wave, lane, b, xg, sp, wave * 3 + sp ST_PASS);
    ST(4); ST(5); ST(6); ST(7);
    ST_FLUSH();
    __builtin_amdgcn_endpgm();
  }
  const int j = idx;
  const int prow = lane >> 3;
  const int ce = (lane & 7) ^ (2 * ((lane >> 4) & 1)), co = ce ^ 4;
  const int fsw = ((r4 >> 1) & 1) | ((g & 1) << 1);
  const int trow = (8 * g + r4) * 128 + q4 * 8;
  auto tfrag = [&](LAS unsigned char* tile, int s, int t) { LAS unsigned char* p = tile + s * 4096 + trow + ((t ^ fsw) << 5); return trfrag(p, p + 512); };
  unsigned tag;
  {
    bf16x8 xf[4]; unsigned ev;
    {
      const unsigned xo = (unsigned)((n & 7) * 1024 + wave * 256 + g * 16), zero = 0;
      asm volatile("global_load_dword %4, %7, %8 sc1\n\tglobal_load_dwordx4 %0, %5, %6\n\tglobal_load_dwordx4 %1, %5, %6 offset:64\n\tglobal_load_dwordx4 %2, %5, %6 offset:128\n\tglobal_load_dwordx4 %3, %5, %6 offset:192"
                   : "=&v"(xf[0]), "=&v"(xf[1]), "=&v"(xf[2]), "=&v"(xf[3]), "=&v"(ev) : "v"(xo), "s"(a.x), "v"(zero), "s"(a.ws) : "memory");
    }
    {
      const unsigned char* src = a.wq + (size_t)(xg * 512 + j * 64) * 2;
      const unsigned ve = (unsigned)((wave * 128 + prow) * 8192 + ce * 16), vo = (unsigned)((wave * 128 + 8 + prow) * 8192 + co * 16);
#pragma unroll
      for (int i = 0; i < 8; ++i) { wpiece(src, ve + i * 16 * 8192, ldsb + T0_OFF + wave * 16384 + i * 2048); wpiece(src, vo + i * 16 * 8192, ldsb + T0_OFF + wave * 16384 + i * 2048 + 1024); }
    }
    asm volatile("s_waitcnt vmcnt(0)" : "+v"(xf[0]), "+v"(xf[1]), "+v"(xf[2]), "+v"(xf[3]), "+v"(ev) :: "memory");
    tag = __builtin_amdgcn_readfirstlane(ev) + 1u;
    const int pl = (g * 8 + (n & 7)) * 16;
    auto proj = [&](LAS unsigned char* tile, LAS unsigned char* dst) {
      f32x4 c[4];
#pragma unroll
      for (int t = 0; t < 4; ++t) c[t] = f32x4{0.f, 0.f, 0.f, 0.f};
#pragma unroll
      for (int s = 0; s < 4; ++s)
#pragma unroll
        for (int t = 0; t < 4; ++t) c[t] = __builtin_amdgcn_mfma_f32_16x16x32_bf16(tfrag(tile, s, t), xf[s], c[t], 0, 0, 0);
      if (n < 8) {
#pragma unroll
        for (int t = 0; t < 4; ++t) *(LAS f32x4*)(dst + t * 512 + pl) = c[t];
      }
    };
    auto red4 = [&](LAS unsigned char* p) { return (*(const LAS f32x4*)p + *(const LAS f32x4*)(p + 2048)) + (*(const LAS f32x4*)(p + 4096) + *(const LAS f32x4*)(p + 6144)); };
    proj(lds + T0_OFF + wave * 16384, lds + QP_OFF + wave * 2048);
    wg_barrier();
    {
      const f32x4 c0 = red4(lds + QP_OFF + wave * 512 + pl);
      const unsigned val = n < 8 ? cvtpk(c0[0], c0[1]) : cvtpk(c0[2], c0[3]);
      put((gu64*)(a.ws + WS_QG) + (size_t)(xg * 8 + (n & 7)) * 256 + j * 32 + wave * 8 + g * 2 + (n >> 3), tag, val);
    }
    ST(1);
    if (j < 7) {
      const size_t col = (size_t)(xg * 224 + j * 32) * 2;
      const unsigned char* se = (ce < 4 ? a.wg : a.wu) + (size_t)(wave * 128 + prow) * (FF * 2) + col + (ce & 3) * 16;
      const unsigned char* so = (co < 4 ? a.wg : a.wu) + (size_t)(wave * 128 + 8 + prow) * (FF * 2) + col + (co & 3) * 16;
#pragma unroll
      for (int i = 0; i < 8; ++i) { wpiece64(se + (size_t)i * 16 * FF * 2, ldsb + T1_OFF + wave * 16384 + i * 2048); wpiece64(so + (size_t)i * 16 * FF * 2, ldsb + T1_OFF + wave * 16384 + i * 2048 + 1024); }
      wait_vm<0>();
      proj(lds + T1_OFF + wave * 16384, lds + GP_OFF + wave * 2048);
    }
    wg_barrier();
    if (j < 7 && wave < 2) {
      const f32x4 cg = red4(lds + GP_OFF + wave * 512 + pl), cu = red4(lds + GP_OFF + (wave + 2) * 512 + pl);
      float hv[4];
#pragma unroll
      for (int k = 0; k < 4; ++k) hv[k] = rbf(sigmoidf_(rbf(cg[k]))) * rbf(cu[k]);
      const unsigned val = n < 8 ? cvtpk(hv[0], hv[1]) : cvtpk(hv[2], hv[3]);
      put((gu64*)(a.ws + WS_HG) + (size_t)(xg * 8 + (n & 7)) * 112 + j * 16 + wave * 8 + g * 2 + (n >> 3), tag, val);
    }
  }
  ST(2);
  stream_split<NA, 4, 0>(a, lds, wsr, wave, lane, j, xg, 3, 12 * NM + wave ST_PASS);
  const int head = xg * 4 + (j >> 1);
  {
    const unsigned char* src = a.wo + ((size_t)(head * 128) * DM + (j & 1) * 256 + wave * 64) * 2;
    const unsigned ve = (unsigned)(prow * 1024 + ce * 16), vo = (unsigned)((8 + prow) * 1024 + co * 16);
#pragma unroll
    for (int i = 0; i < 8; ++i) { wpiece(src, ve + i * 16 * 1024, ldsb + T0_OFF + wave * 16384 + i * 2048); wpiece(src, vo + i * 16 * 1024, ldsb + T0_OFF + wave * 16384 + i * 2048 + 1024); }
  }
  {
    const unsigned char* src = a.wd + ((size_t)(xg * 224) * DM + j * 64) * 2;
#pragma unroll
    for (int i = 0; i < 7; ++i) { const int pc = wave * 7 + i; wpiece(src, (unsigned)((pc * 8 + prow) * 1024 + ((pc & 1) ? co : ce) * 16), ldsb + T1_OFF + pc * 1024); }
  }
  float gate;
  {
    const v4u xg8 = *(const GAS v4u*)(a.x + (size_t)j * 1024 + lane * 16), gw8 = *(const GAS v4u*)(a.gw + lane * 16);
    float s = 0.f;
#pragma unroll
    for (int k = 0; k < 4; ++k) {
      s += rbf(bf2f(xg8[k] & 0xffffu) * bf2f(gw8[k] & 0xffffu));
      s += rbf(bf2f(xg8[k] >> 16) * bf2f(gw8[k] >> 16));
    }
    s += dpp<0xB1>(s); s += dpp<0x4E>(s); s += dpp<0x141>(s); s += dpp<0x128>(s);
    s = xrow_sum(s);
    gate = rbf(sigmoidf_(rbf(s)));
  }
  bf16x8 wf[16];
  {
    v4u hy[7];
    const unsigned hg = (unsigned)WS_HG + (unsigned)(((xg * 8 + (n & 7)) * 112 + g * 4 + (n >> 3) * 2) * 8);
#pragma unroll 1
    for (unsigned it = 0; it < SPIN_ITERS; ++it) {
      bool ok = true;
#pragma unroll
      for (int s = 0; s < 7; ++s) { hy[s] = ld2g(wsr, hg + s * 128, 0); ok &= hy[s].y == tag && hy[s].w == tag; }
      if (__all(ok)) break;
      __builtin_amdgcn_s_sleep(8);
      asm volatile("" ::: "memory");
    }
    wait_vm<0>();
    wg_barrier();
#pragma unroll
    for (int s = 0; s < 4; ++s)
#pragma unroll
      for (int t = 0; t < 4; ++t) wf[s * 4 + t] = tfrag(lds + T0_OFF + wave * 16384, s, t);
    f32x4 c0 = {0.f, 0.f, 0.f, 0.f};
#pragma unroll
    for (int s = 0; s < 7; ++s) {
      const unsigned o0 = (unsigned)__builtin_amdgcn_mov_dpp((int)hy[s].x, 0x128, 0xf, 0xf, true), o1 = (unsigned)__builtin_amdgcn_mov_dpp((int)hy[s].z, 0x128, 0xf, 0xf, true);
      const v4u hf = n < 8 ? v4u{hy[s].x, hy[s].z, o0, o1} : v4u{o0, o1, hy[s].x, hy[s].z};
      c0 = __builtin_amdgcn_mfma_f32_16x16x32_bf16(tfrag(lds + T1_OFF, s, wave), __builtin_bit_cast(bf16x8, hf), c0, 0, 0, 0);
    }
    st2g(wsr, (unsigned)WS_DG + (unsigned)((((n & 7) * 32 + j * 4 + wave) * 128 + xg * 16 + g * 4 + (n >> 3) * 2) * 8), tag, n < 8 ? c0[0] : c0[2], n < 8 ? c0[1] : c0[3]);
  }
  ST(4);
  {
    const unsigned grp = (unsigned)WS_PG + (unsigned)((head * 8 + wave * 2) * NSP) * (PG_REC * 8);
    const unsigned vpay = grp + lane * 16, vml = lane < 8 ? grp + lane * (PG_REC * 8) + 1024 : OOB;
    float pm_[8], pl_[8], p0_[8], p1_[8];
#pragma unroll
    for (int s = 0; s < 8; ++s) { pm_[s] = 0.f; pl_[s] = 1.f; p0_[s] = 0.f; p1_[s] = 0.f; }
    unsigned missing = 255u, hot = 0x88u;
#pragma unroll 1
    for (unsigned it = 0; it < SPIN_ITERS; ++it) {
      v4u r[9];
      const unsigned want = missing & hot;
#pragma unroll
      for (int s = 0; s < 8; ++s) r[s] = ld2g(wsr, vpay + s * (PG_REC * 8), (want >> s) & 1 ? 0u : OOB);
      r[8] = ld2g(wsr, vml, 0);
      asm volatile("" ::: "memory");
      const unsigned seen = (unsigned)__ballot(r[8].y == tag && r[8].w == tag) & 255u, ml = seen & want;
#pragma unroll
      for (int s = 0; s < 8; ++s) if ((ml >> s) & 1) {
        if (__all(r[s].y == tag && r[s].w == tag)) {
          p0_[s] = __uint_as_float(r[s].x); p1_[s] = __uint_as_float(r[s].z);
          pm_[s] = __uint_as_float(__builtin_amdgcn_readlane(r[8].x, s)); pl_[s] = __uint_as_float(__builtin_amdgcn_readlane(r[8].z, s));
          missing &= ~(1u << s);
        }
      }
      if (!missing) break;
      if (seen & 0x77u) hot = 255u; else __builtin_amdgcn_s_sleep(SLEEP0);
      asm volatile("" ::: "memory");
    }
    ST(5);
#pragma unroll
    for (int bb = 0; bb < 2; ++bb) {
      const float MM = fmaxf(fmaxf(pm_[4 * bb], pm_[4 * bb + 1]), fmaxf(pm_[4 * bb + 2], pm_[4 * bb + 3]));
      float LL = 0.f, y0 = 0.f, y1 = 0.f;
#pragma unroll
      for (int s = 4 * bb; s < 4 * bb + 4; ++s) { const float wt = __builtin_amdgcn_exp2f(pm_[s] - MM); LL = fmaf(wt, pl_[s], LL); y0 = fmaf(wt, p0_[s], y0); y1 = fmaf(wt, p1_[s], y1); }
      const float inv = __builtin_amdgcn_rcpf(LL);
      *(LAS unsigned*)(lds + O_OFF + (wave * 2 + bb) * O_PITCH + lane * 4) = cvtpk(y0 * inv, y1 * inv);
    }
  }
  wg_barrier();
  {
    f32x4 c[4];
#pragma unroll
    for (int t = 0; t < 4; ++t) c[t] = f32x4{0.f, 0.f, 0.f, 0.f};
    bf16x8 of[4];
#pragma unroll
    for (int s = 0; s < 4; ++s) of[s] = *(const LAS bf16x8*)(lds + O_OFF + (n & 7) * O_PITCH + s * 64 + g * 16);
#pragma unroll
    for (int s = 0; s < 4; ++s)
#pragma unroll
      for (int t = 0; t < 4; ++t) c[t] = __builtin_amdgcn_mfma_f32_16x16x32_bf16(wf[s * 4 + t], of[s], c[t], 0, 0, 0);
    const unsigned ag = (unsigned)WS_AG + (unsigned)((((n & 7) * 32 + (j & 1) * 16 + wave * 4) * 512 + head * 16 + g * 4 + (n >> 3) * 2) * 8);
#pragma unroll
    for (int t = 0; t < 4; ++t) st2g(wsr, ag + t * 4096, tag, n < 8 ? c[t][0] : c[t][2], n < 8 ? c[t][1] : c[t][3]);
  }
  ST(6);
  {
    const int c16 = xg * 4 + wave;
    auto red8 = [&](float v) { v += dpp<0x128>(v); return xrow_sum(v); };
    const unsigned ag = (unsigned)WS_AG + (unsigned)((j * 32 + c16) * 4096 + lane * 16);
    const unsigned dg = (unsigned)WS_DG + (unsigned)((j * 32 + c16) * 1024 + lane * 16);
    float a0[5], a1[5];
#pragma unroll
    for (int k = 0; k < 5; ++k) { a0[k] = 0.f; a1[k] = 0.f; }
    unsigned missing = 31u;
#pragma unroll 1
    for (unsigned it = 0; it < SPIN_ITERS; ++it) {
      v4u r[5];
#pragma unroll
      for (int k = 0; k < 4; ++k) r[k] = ld2g(wsr, ag + k * 1024, (missing >> k) & 1 ? 0u : OOB);
      r[4] = ld2g(wsr, dg, (missing >> 4) & 1 ? 0u : OOB);
      asm volatile("" ::: "memory");
#pragma unroll
      for (int k = 0; k < 5; ++k) if ((missing >> k) & 1) {
        if (__all(r[k].y == tag && r[k].w == tag)) { a0[k] = __uint_as_float(r[k].x); a1[k] = __uint_as_float(r[k].z); missing &= ~(1u << k); }
      }
      if (!missing) break;
      asm volatile("" ::: "memory");
    }
    const float bm0 = rbf(red8(a0[4])), bm1 = rbf(red8(a1[4]));
    const float A0 = rbf(red8((a0[0] + a0[1]) + (a0[2] + a0[3]))), A1 = rbf(red8((a1[0] + a1[1]) + (a1[2] + a1[3])));
    const float g1 = rbf(1.f - gate);
    const float r0 = rbf(gate * A0) + rbf(g1 * bm0), r1 = rbf(gate * A1) + rbf(g1 * bm1);
    if (lane < 8) *(GAS unsigned*)(a.out + j * DM + c16 * 16 + lane * 2) = cvtpk(r0, r1);
    if (bid == 0 && wave == 0 && lane == 0) __hip_atomic_store((gu32*)(a.ws + WS_E), tag, RLX_AGENT);
  }
  ST(7);
  ST_FLUSH();
}

extern "C" void kernel_launch(void* const* d_in, const int* in_sizes, int n_in, void* d_out, int out_size, void* d_ws, size_t ws_size, hipStream_t stream) {
  Args a;
  a.x = (const unsigned char*)d_in[0]; a.K = (const unsigned char*)d_in[1]; a.V = (const unsigned char*)d_in[2];
  a.wq = (const unsigned char*)d_in[3]; a.wo = (const unsigned char*)d_in[4];
  a.wg = (const unsigned char*)d_in[6]; a.wu = (const unsigned char*)d_in[7]; a.wd = (const unsigned char*)d_in[8]; a.gw = (const unsigned char*)d_in[9];
  a.ws = (unsigned char*)d_ws; a.out = (u16*)d_out;
  (void)hipFuncSetAttribute((const void*)fused_kernel, hipFuncAttributeMaxDynamicSharedMemorySize, LDS_BYTES);
  fused_kernel<<<256, 320, LDS_BYTES, stream>>>(a);
}
```

```cpp
#include <hip/hip_runtime.h>
#include <hip/hip_bf16.h>
#include <cstdint>

#define GAS __attribute__((address_space(1)))
#define LAS __attribute__((address_space(3)))
typedef unsigned v4u __attribute__((ext_vector_type(4)));
typedef float f32x4 __attribute__((ext_vector_type(4)));
typedef float f32x2 __attribute__((ext_vector_type(2)));
typedef __bf16 bf16x8 __attribute__((ext_vector_type(8)));
typedef short s16x4 __attribute__((ext_vector_type(4)));
typedef unsigned short u16;
typedef unsigned long long u64;
typedef GAS u64 gu64;
typedef GAS unsigned gu32;
typedef __amdgpu_buffer_rsrc_t rsrc_t;
#define RLX_AGENT __ATOMIC_RELAXED, __HIP_MEMORY_SCOPE_AGENT

#define GU_AUX " nt"
#define EXP 0
constexpr int DM = 512, HKV = 8, SK = 8192, FF = 1792;

__device__ __forceinline__ float bf2f(unsigned v) { return __builtin_bit_cast(float, v << 16); }
__device__ __forceinline__ unsigned cvtpk(float lo, float hi) { unsigned r; asm("v_cvt_pk_bf16_f32 %0, %1, %2" : "=v"(r) : "v"(lo), "v"(hi)); return r; }
__device__ __forceinline__ float rbf(float x) { return __builtin_bit_cast(float, cvtpk(x, x) << 16); }
template <int CTRL> __device__ __forceinline__ float dpp(float x) {
  return __builtin_bit_cast(float, __builtin_amdgcn_mov_dpp(__builtin_bit_cast(int, x), CTRL, 0xf, 0xf, true));
}
__device__ __forceinline__ float xrow_max(float x) {
  auto s = __builtin_amdgcn_permlane16_swap(__float_as_uint(x), __float_as_uint(x), false, false);
  x = fmaxf(__uint_as_float(s[0]), __uint_as_float(s[1]));
  auto t = __builtin_amdgcn_permlane32_swap(__float_as_uint(x), __float_as_uint(x), false, false);
  return fmaxf(__uint_as_float(t[0]), __uint_as_float(t[1]));
}
__device__ __forceinline__ float xrow_sum(float x) {
  auto s = __builtin_amdgcn_permlane16_swap(__float_as_uint(x), __float_as_uint(x), false, false);
  x = __uint_as_float(s[0]) + __uint_as_float(s[1]);
  auto t = __builtin_amdgcn_permlane32_swap(__float_as_uint(x), __float_as_uint(x), false, false);
  return __uint_as_float(t[0]) + __uint_as_float(t[1]);
}
template <int N> __device__ __forceinline__ void wait_vm() { asm volatile("s_waitcnt vmcnt(%0)" ::"n"(N) : "memory"); }
__device__ __forceinline__ void wg_barrier() { asm volatile("s_waitcnt lgkmcnt(0)\n\ts_barrier" ::: "memory"); }
__device__ __forceinline__ u64 rt() { return __builtin_amdgcn_s_memrealtime(); }
__device__ __forceinline__ float sigmoidf_(float v) { return 1.f / (1.f + __expf(-v)); }

__device__ __forceinline__ void fill8(const unsigned char* base, const unsigned (&v)[4], unsigned dst) {
  unsigned keep;
  asm volatile(
      "s_mov_b32 %0, m0\n\t"
      "s_mov_b32 m0, %7\n\ts_nop 0\n\t"
      "global_load_lds_dwordx4 %1, %5 nt\n\t"
      "global_load_lds_dwordx4 %2, %5 offset:1024 nt\n\t"
      "global_load_lds_dwordx4 %3, %5 offset:2048 nt\n\t"
      "global_load_lds_dwordx4 %4, %5 offset:3072 nt\n\t"
      "s_bitset1_b32 m0, 12\n\ts_nop 0\n\t"
      "global_load_lds_dwordx4 %1, %6 nt\n\t"
      "global_load_lds_dwordx4 %2, %6 offset:1024 nt\n\t"
      "global_load_lds_dwordx4 %3, %6 offset:2048 nt\n\t"
      "global_load_lds_dwordx4 %4, %6 offset:3072 nt\n\t"
      "s_mov_b32 m0, %0"
      : "=&s"(keep)
      : "v"(v[0]), "v"(v[1]), "v"(v[2]), "v"(v[3]), "s"(base), "s"(base + 4096), "s"(dst)
      : "memory");
}
__device__ __forceinline__ void wpiece(const void* sbase, unsigned voff, unsigned dst) {
  unsigned keep;
  asm volatile("s_mov_b32 %0, m0\n\ts_mov_b32 m0, %3\n\ts_nop 0\n\tglobal_load_lds_dwordx4 %1, %2 nt\n\ts_mov_b32 m0, %0"
               : "=&s"(keep) : "v"(voff), "s"(sbase), "s"(dst) : "memory");
}
__device__ __forceinline__ void wpiece64(const void* vaddr, unsigned dst) {
  unsigned keep;
  asm volatile("s_mov_b32 %0, m0\n\ts_mov_b32 m0, %2\n\ts_nop 0\n\tglobal_load_lds_dwordx4 %1, off" GU_AUX "\n\ts_mov_b32 m0, %0"
               : "=&s"(keep) : "v"(vaddr), "s"(dst) : "memory");
}
__device__ __forceinline__ bf16x8 trfrag(LAS unsigned char* p0, LAS unsigned char* p1) {
  const s16x4 a = __builtin_amdgcn_ds_read_tr16_b64_v4i16((LAS s16x4*)p0), b = __builtin_amdgcn_ds_read_tr16_b64_v4i16((LAS s16x4*)p1);
  return __builtin_bit_cast(bf16x8, __builtin_shufflevector(a, b, 0, 1, 2, 3, 4, 5, 6, 7));
}
__device__ __forceinline__ void put(gu64* p, unsigned tag, unsigned val) { __hip_atomic_store(p, ((u64)tag << 32) | val, RLX_AGENT); }
__device__ __forceinline__ v4u ld2g(rsrc_t r, unsigned voff, unsigned soff) { return __builtin_amdgcn_raw_buffer_load_b128(r, voff, soff, 16); }
__device__ __forceinline__ void st2g(rsrc_t r, unsigned voff, unsigned tag, float v0, float v1) {
  __builtin_amdgcn_raw_buffer_store_b128(v4u{__float_as_uint(v0), tag, __float_as_uint(v1), tag}, r, voff, 0, 16);
}
#define PLD(r, v, m, off) "s_mov_b64 exec, " m "\n\ts_cbranch_execz 2\n\tbuffer_load_dwordx4 " r ", " v ", %[srd], 0 offen offset:" off " sc1\n\t"
template <int STRIDE> __device__ __forceinline__ void poll7(v4u (&r)[7], const u64 (&m)[7], const v4u& srd, unsigned va, unsigned vb, unsigned vc) {
  asm volatile(PLD("%0", "%[va]", "%[m0]", "0") PLD("%1", "%[va]", "%[m1]", "%[s1]") PLD("%2", "%[va]", "%[m2]", "%[s2]")
               PLD("%3", "%[vb]", "%[m3]", "0") PLD("%4", "%[vb]", "%[m4]", "%[s1]") PLD("%5", "%[vb]", "%[m5]", "%[s2]")
               PLD("%6", "%[vc]", "%[m6]", "0") "s_mov_b64 exec, -1\n\ts_waitcnt vmcnt(0)"
               : "+v"(r[0]), "+v"(r[1]), "+v"(r[2]), "+v"(r[3]), "+v"(r[4]), "+v"(r[5]), "+v"(r[6])
               : [va] "v"(va), [vb] "v"(vb), [vc] "v"(vc), [srd] "s"(srd), [m0] "s"(m[0]), [m1] "s"(m[1]), [m2] "s"(m[2]), [m3] "s"(m[3]), [m4] "s"(m[4]), [m5] "s"(m[5]), [m6] "s"(m[6]),
                 [s1] "n"(STRIDE), [s2] "n"(2 * STRIDE)
               : "memory");
}
__device__ __forceinline__ void poll4(v4u (&r)[4], const u64 (&m)[4], const v4u& srd, unsigned va) {
  asm volatile(PLD("%0", "%[va]", "%[m0]", "0") PLD("%1", "%[va]", "%[m1]", "1024") PLD("%2", "%[va]", "%[m2]", "2048") PLD("%3", "%[va]", "%[m3]", "3072")
               "s_mov_b64 exec, -1\n\ts_waitcnt vmcnt(0)"
               : "+v"(r[0]), "+v"(r[1]), "+v"(r[2]), "+v"(r[3])
               : [va] "v"(va), [srd] "s"(srd), [m0] "s"(m[0]), [m1] "s"(m[1]), [m2] "s"(m[2]), [m3] "s"(m[3])
               : "memory");
}
__device__ __forceinline__ u64 waiting(const v4u& r, unsigned tag) { return __builtin_amdgcn_uicmp(r.y, tag, 33) | __builtin_amdgcn_uicmp(r.w, tag, 33); }
constexpr unsigned OOB = 0x01000000u;
constexpr unsigned SPIN_ITERS = 1u << 17;

#define INFL 24
#define PINFL 16
#define NREG 3
#define SLEEP0 4
#define ROTMASK 15
#define CODE_LINES 168
#define CSLEEP 0
#define PT 1
#define HX 0
#define SPEC 6
#define TQUIET 3000
#define QDELAY 300
#define QSLEEP 16
#define WARM 0

constexpr int SLOT = 8192, RING = 4 * SLOT;
constexpr int MRG_OFF = 4 * RING, MRG_REC = 132;
constexpr int QP_OFF = MRG_OFF + 4 * 4 * MRG_REC * 4, GP_OFF = QP_OFF + 8192;
constexpr int O_OFF = MRG_OFF, O_PITCH = 272;
constexpr int Q_OFF = GP_OFF + 8192;
constexpr int TAG_OFF = Q_OFF + 1024;
constexpr int T0_OFF = 0, T1_OFF = 65536;
constexpr int LDS_BYTES = TAG_OFF + 16;
static_assert(LDS_BYTES <= 163840 && LDS_BYTES > 81920, "one workgroup per CU");

constexpr int PG_REC = 1152;
constexpr unsigned WS_E = 0;
constexpr unsigned WS_QG = 4096;
constexpr unsigned WS_HG = WS_QG + 8 * 8 * 256 * 8;
constexpr unsigned WS_DG = WS_HG + 65536;
constexpr unsigned WS_PG = WS_DG + 8 * 32 * 1024;
constexpr unsigned WS_AG = WS_PG + 32 * 24 * PG_REC;
constexpr unsigned WS_END = WS_AG + 8 * 32 * 4096;
constexpr size_t WS_ST = (size_t)64 << 20;

struct Args { const unsigned char *K, *V; unsigned char* ws; const unsigned char *x, *wq, *wo, *wg, *wu, *wd, *gw; u16* out; };

#define ST(i) do {} while (0)
#define ST_FLUSH() do {} while (0)

__global__ void __launch_bounds__(512, 2) fused_kernel(Args a) {
  extern __shared__ __attribute__((aligned(16))) unsigned char lds_[];
  LAS unsigned char* lds = (LAS unsigned char*)lds_;
  const int tid = threadIdx.x, lane = tid & 63, wave = __builtin_amdgcn_readfirstlane(tid >> 6);
  const int bid = blockIdx.x, xg = bid & 7, idx = bid >> 3;
  const int n = lane & 15, g = lane >> 4, q4 = n & 3, r4 = n >> 2;
  if (__builtin_expect(idx >= 8 && wave < 4, 1)) {
    const int kvh = xg, b = idx & 7, sp = (idx >> 3) - 1, pair = b * HKV + kvh, gw = wave * 3 + sp, NU = gw < 4 ? 22 : 21;
    const int rot = pair & ROTMASK;
    const unsigned ring = (unsigned)(size_t)lds + wave * RING;
    const size_t toff = (size_t)(pair * 256 + gw) * 8192;
    const unsigned char* kb = a.K + toff;
    const unsigned char* vb = a.V + toff;
    unsigned voff[4];
#pragma unroll
    for (int i = 0; i < 4; ++i) voff[i] = (unsigned)(g * 256 + ((n ^ (g << 2) ^ (((i & 1) << 1) | (i >> 1))) * 16));
    auto uoff = [&](int u) { int t = u + rot; if (t >= NU) t -= NU; return (unsigned)t * (12 * 8192); };
    fill8(kb + uoff(0), voff, ring);
    fill8(vb + uoff(0), voff, ring + SLOT);
    ST(1);
    fill8(kb + uoff(1), voff, ring + 2 * SLOT);
    const rsrc_t wsr = __builtin_amdgcn_make_buffer_rsrc(a.ws, 0, (int)WS_END, 0x00020000);
    const unsigned rr = ((r4 & 1) << 1) | (r4 >> 1), rg = ((g & 1) << 1) | (g >> 1);
    const unsigned kbase = ring + r4 * 1024 + q4 * 256 + (g ^ q4) * 64 + rr * 16;
    const unsigned vbase = ring + SLOT + g * 1024 + r4 * 256 + (((q4 >> 1) ^ (r4 << 2) ^ rg) * 16) + (q4 & 1) * 8;
    auto readK = [&](unsigned so, bf16x8 (&kf)[8]) {
#pragma unroll
      for (int t = 0; t < 4; ++t) {
        LAS unsigned char* p = (LAS unsigned char*)(size_t)(kbase ^ (so + 16 * t));
        kf[t] = *(const LAS bf16x8*)p; kf[4 + t] = *(const LAS bf16x8*)(p + 4096);
      }
      asm volatile("s_waitcnt lgkmcnt(0)" : "+v"(kf[0]), "+v"(kf[1]), "+v"(kf[2]), "+v"(kf[3]), "+v"(kf[4]), "+v"(kf[5]), "+v"(kf[6]), "+v"(kf[7]) :: "memory");
    };
    auto readV = [&](unsigned so, bf16x8 (&vf)[8]) {
#pragma unroll
      for (int e = 0; e < 8; ++e) { LAS unsigned char* p = (LAS unsigned char*)(size_t)(vbase ^ (so + 32 * e)); vf[e] = trfrag(p, p + 4096); }
      asm volatile("s_waitcnt lgkmcnt(0)" : "+v"(vf[0]), "+v"(vf[1]), "+v"(vf[2]), "+v"(vf[3]), "+v"(vf[4]), "+v"(vf[5]), "+v"(vf[6]), "+v"(vf[7]) :: "memory");
    };
    int pass = (WARM & 1) ? 0 : 1;
#pragma unroll 1
    do {
      asm volatile("" : "+s"(pass));
      f32x4 acc[8];
#pragma unroll
      for (int e = 0; e < 8; ++e) acc[e] = f32x4{0.f, 0.f, 0.f, 0.f};
      float m = -INFINITY, l = 0.f;
      unsigned tag = 0;
      if (pass) {
        wait_vm<PINFL>();
        fill8(vb + uoff(1), voff, ring + 3 * SLOT);
        bf16x8 kr[NREG][8], vr[NREG][8];
#pragma unroll
        for (int u = 0; u < NREG; ++u) {
          const unsigned so = (u & 1) * 2 * SLOT;
          wait_vm<PINFL>();
          readK(so, kr[u]);
          fill8(kb + uoff(u + 2), voff, ring + so);
          wait_vm<PINFL>();
          readV(so, vr[u]);
          fill8(vb + uoff(u + 2), voff, ring + so + SLOT);
        }
        ST(2);
        if (!(EXP & 4)) wg_barrier();
        bf16x8 qf[4];
#pragma unroll
        for (int t = 0; t < 4; ++t) qf[t] = *(const LAS bf16x8*)(lds + Q_OFF + q4 * 256 + g * 64 + t * 16);
        tag = __builtin_amdgcn_readfirstlane(*(const LAS unsigned*)(lds + TAG_OFF));
        constexpr float C2 = 0.088388347648318440f * 1.4426950408889634f;
        auto scores = [&](const bf16x8 (&kf)[8]) -> bf16x8 {
          f32x4 s0 = {0.f, 0.f, 0.f, 0.f}, s1 = {0.f, 0.f, 0.f, 0.f};
#pragma unroll
          for (int t = 0; t < 4; ++t) {
            s0 = __builtin_amdgcn_mfma_f32_16x16x32_bf16(kf[t], qf[t], s0, 0, 0, 0);
            s1 = __builtin_amdgcn_mfma_f32_16x16x32_bf16(kf[4 + t], qf[t], s1, 0, 0, 0);
          }
          float s[8];
#pragma unroll
          for (int j = 0; j < 4; ++j) { s[j] = s0[j] * C2; s[4 + j] = s1[j] * C2; }
          float pm = fmaxf(fmaxf(fmaxf(s[0], s[1]), fmaxf(s[2], s[3])), fmaxf(fmaxf(s[4], s[5]), fmaxf(s[6], s[7])));
          pm = xrow_max(pm);
          if (__any(pm > m)) {
            const float mn = fmaxf(m, pm);
            const float alpha = __builtin_amdgcn_exp2f(m - mn);
            m = mn; l *= alpha;
            const float a0 = dpp<0x00>(alpha), a1 = dpp<0x55>(alpha), a2 = dpp<0xAA>(alpha), a3 = dpp<0xFF>(alpha);
#pragma unroll
            for (int e = 0; e < 8; ++e) { acc[e][0] *= a0; acc[e][1] *= a1; acc[e][2] *= a2; acc[e][3] *= a3; }
          }
          float p[8];
#pragma unroll
          for (int j = 0; j < 8; ++j) p[j] = __builtin_amdgcn_exp2f(s[j] - m);
          l += ((p[0] + p[1]) + (p[2] + p[3])) + ((p[4] + p[5]) + (p[6] + p[7]));
          const v4u pk = {cvtpk(p[0], p[1]), cvtpk(p[2], p[3]), cvtpk(p[4], p[5]), cvtpk(p[6], p[7])};
          return __builtin_bit_cast(bf16x8, pk);
        };
        auto pv = [&](const bf16x8 pf, const bf16x8 (&vf)[8]) {
#pragma unroll
          for (int e = 0; e < 8; ++e) acc[e] = __builtin_amdgcn_mfma_f32_16x16x32_bf16(pf, vf[e], acc[e], 0, 0, 0);
        };
#pragma unroll
        for (int u = 0; u < NREG; ++u) pv(scores(kr[u]), vr[u]);
#pragma unroll 1
        for (int u = NREG; u < NU; ++u) {
          const unsigned so = (u & 1) * 2 * SLOT;
          const bool more = u + 2 < NU;
          if (more) wait_vm<INFL>(); else if (u + 1 < NU) wait_vm<24>(); else wait_vm<8>();
          bf16x8 kf[8];
          readK(so, kf);
          if (more) fill8(kb + uoff(u + 2), voff, ring + so);
          const bf16x8 pf = scores(kf);
          if (more) wait_vm<INFL>(); else if (u + 1 < NU) wait_vm<16>(); else wait_vm<0>();
          bf16x8 vf[8];
          readV(so, vf);
          if (more) fill8(vb + uoff(u + 2), voff, ring + so + SLOT);
          pv(pf, vf);
        }
        ST(3);
      }
      l = xrow_sum(l);
      LAS float* mrg = (LAS float*)(lds + MRG_OFF);
      {
        const f32x4 e0 = g == 0 ? acc[0] : g == 1 ? acc[2] : g == 2 ? acc[4] : acc[6], e1 = g == 0 ? acc[1] : g == 1 ? acc[3] : g == 2 ? acc[5] : acc[7];
        LAS float* w = mrg + wave * 4 * MRG_REC + 4 + 32 * g + n;
#pragma unroll
        for (int h = 0; h < 4; ++h) { w[h * MRG_REC] = e0[h]; w[h * MRG_REC + 16] = e1[h]; }
      }
      if (lane < 4) *(LAS f32x2*)(mrg + (wave * 4 + lane) * MRG_REC) = f32x2{m, l};
      if (pass) wg_barrier();
      f32x2 ml[4], ov[4];
#pragma unroll
      for (int w = 0; w < 4; ++w) { const LAS float* rec = mrg + (w * 4 + wave) * MRG_REC; ml[w] = *(const LAS f32x2*)rec; ov[w] = *(const LAS f32x2*)(rec + 4 + 2 * lane); }
      const float M = fmaxf(fmaxf(ml[0][0], ml[1][0]), fmaxf(ml[2][0], ml[3][0]));
      float L = 0.f, o0 = 0.f, o1 = 0.f;
#pragma unroll
      for (int w = 0; w < 4; ++w) { const float wt = __builtin_amdgcn_exp2f(ml[w][0] - M); L = fmaf(wt, ml[w][1], L); o0 = fmaf(wt, ov[w][0], o0); o1 = fmaf(wt, ov[w][1], o1); }
      const unsigned mine = WS_PG + (unsigned)((kvh * 4 + wave) * 24 + b * 3 + sp) * PG_REC + (pass ? 0u : OOB);
      st2g(wsr, mine + lane * 16, tag, o0, o1);
      if (lane == 0) st2g(wsr, mine + 1024, tag, M, L);
    } while (++pass < 2);
    ST(4);
    ST_FLUSH();
    return;
  }
  if (EXP & 4) { ST_FLUSH(); return; }
  if (idx >= 8) {
    if (wave > 4) return;
    const u64 tstart = rt();
    __builtin_amdgcn_s_setprio(3);
    const rsrc_t wsr = __builtin_amdgcn_make_buffer_rsrc(a.ws, 0, (int)WS_END, 0x00020000);
    const unsigned tag = __builtin_amdgcn_readfirstlane(__hip_atomic_load((const gu32*)(a.ws + WS_E), RLX_AGENT)) + 1u;
    if (lane == 0) *(LAS unsigned*)(lds + TAG_OFF) = tag;
    ST(1);
    const unsigned qg = WS_QG + (unsigned)((xg * 8 + (idx & 7)) * 2048 + lane * 16);
    while (rt() - tstart < QDELAY) __builtin_amdgcn_s_sleep(8);
    v4u y0, y1;
#pragma unroll 1
    for (unsigned it = 0; it < SPIN_ITERS; ++it) {
      y0 = ld2g(wsr, qg, 0); y1 = ld2g(wsr, qg + 1024, 0);
      if (__all(y0.y == tag && y0.w == tag && y1.y == tag && y1.w == tag)) break;
      __builtin_amdgcn_s_sleep(QSLEEP);
      asm volatile("" ::: "memory");
    }
    typedef unsigned v2u __attribute__((ext_vector_type(2)));
    *(LAS v2u*)(lds + Q_OFF + lane * 8) = v2u{y0.x, y0.z};
    *(LAS v2u*)(lds + Q_OFF + 512 + lane * 8) = v2u{y1.x, y1.z};
    if (PT & 2) {
#pragma unroll
      for (int h = 0; h < 4; ++h) { const unsigned mine = WS_PG + (unsigned)((xg * 4 + h) * 24 + (idx & 7) * 3 + (idx >> 3) - 1) * PG_REC; st2g(wsr, mine + lane * 16, ~tag, 0.f, 0.f); if (lane == 0) st2g(wsr, mine + 1024, ~tag, 0.f, 0.f); }
      wait_vm<0>();
    }
    ST(2);
    wg_barrier();
    ST_FLUSH();
    return;
  }
  if (wave >= 4) {
    const int gi = wave - 4;
    const u64 tstart = rt();
    const rsrc_t wsr = __builtin_amdgcn_make_buffer_rsrc(a.ws, 0, (int)WS_END, 0x00020000);
    const unsigned tag = __builtin_amdgcn_readfirstlane(__hip_atomic_load((const gu32*)(a.ws + WS_E), RLX_AGENT)) + 1u;
    if (lane == 0) *(volatile LAS unsigned*)(lds + TAG_OFF + gi * 4) = 0u;
    asm volatile("" :: "s"(tag) : "memory");
    wg_barrier(); wg_barrier(); wg_barrier();
    __builtin_amdgcn_s_setprio(3);
    const unsigned grp = WS_PG + (unsigned)(((xg ^ HX) * 4 + (idx >> 1)) * 24 + gi * 6) * PG_REC;
    const unsigned vpay = grp + lane * 16, vml = grp + (lane < 6 ? lane : 0) * PG_REC + 1024;
    const v4u srd = {(unsigned)(size_t)a.ws, (unsigned)((size_t)a.ws >> 32) & 0xffffu, WS_END, 0x00020000u};
    while (rt() - tstart < TQUIET) __builtin_amdgcn_s_sleep(32);
    v4u r[7]; u64 m[7];
#pragma unroll
    for (int s = 0; s < 7; ++s) { r[s] = v4u{0u, ~tag, 0u, ~tag}; m[s] = s < 6 ? ~0ull : 63ull; }
    unsigned done = 0;
#pragma unroll 1
    for (unsigned it = 0; it < SPIN_ITERS; ++it) {
      poll7<PG_REC>(r, m, srd, vpay, vpay + 3 * PG_REC, vml);
      m[6] = waiting(r[6], tag) & 63ull;
      if (m[6] == 63ull) {
#pragma unroll
        for (int s = 0; s < 6; ++s) m[s] = 0;
        __builtin_amdgcn_s_sleep(SLEEP0);
        continue;
      }
#pragma unroll
      for (int s = 0; s < 6; ++s) m[s] = waiting(r[s], tag);
      const unsigned ready = ((m[0] | m[1] | m[2] | (m[6] & 7ull)) ? 0u : 1u) | ((m[3] | m[4] | m[5] | (m[6] & 56ull)) ? 0u : 2u);
      if (__builtin_popcountll(m[6]) > SPEC) {
#pragma unroll
        for (int s = 0; s < 6; ++s) m[s] = (m[6] >> s) & 1 ? 0ull : m[s];
      }
      if (ready == done) continue;
#pragma unroll
      for (int q = 0; q < 2; ++q) if (((ready & ~done) >> q) & 1) {
        float pm[3], pl[3];
#pragma unroll
        for (int k = 0; k < 3; ++k) { pm[k] = __uint_as_float(__builtin_amdgcn_readlane(r[6].x, 3 * q + k)); pl[k] = __uint_as_float(__builtin_amdgcn_readlane(r[6].z, 3 * q + k)); }
        const float MM = fmaxf(fmaxf(pm[0], pm[1]), pm[2]);
        float LL = 0.f, y0 = 0.f, y1 = 0.f;
#pragma unroll
        for (int k = 0; k < 3; ++k) { const float wt = __builtin_amdgcn_exp2f(pm[k] - MM); LL = fmaf(wt, pl[k], LL); y0 = fmaf(wt, __uint_as_float(r[3 * q + k].x), y0); y1 = fmaf(wt, __uint_as_float(r[3 * q + k].z), y1); }
        const float inv = __builtin_amdgcn_rcpf(LL);
        *(volatile LAS unsigned*)(lds + O_OFF + (gi * 2 + q) * O_PITCH + lane * 4) = cvtpk(y0 * inv, y1 * inv);
      }
      done = ready;
      *(volatile LAS unsigned*)(lds + TAG_OFF + gi * 4) = done << (2 * gi);
      if (done == 3u) break;
    }
    ST(5);
    if (bid == 0 && gi == 0) {
#pragma unroll 1
      for (unsigned it = 0; it < SPIN_ITERS; ++it) {
        bool ok = true;
#pragma unroll
        for (int k = 0; k < 3; ++k) { const int w = k * 64 + lane; ok &= ld2g(wsr, WS_PG + (unsigned)((w / 24) * 96 + w % 24) * PG_REC + 1024, 0).y == tag; }
        if (__all(ok)) break;
        asm volatile("" ::: "memory");
      }
      if (lane == 0) __hip_atomic_store((gu32*)(a.ws + WS_E), tag, RLX_AGENT);
    }
    ST_FLUSH();
    return;
  }
  const unsigned ldsb = (unsigned)(size_t)lds;
  const rsrc_t wsr = __builtin_amdgcn_make_buffer_rsrc(a.ws, 0, (int)WS_END, 0x00020000);
  const int j = idx;
  const int prow = lane >> 3;
  const int ce = (lane & 7) ^ (2 * ((lane >> 4) & 1)), co = ce ^ 4;
  const int fsw = ((r4 >> 1) & 1) | ((g & 1) << 1);
  const int trow = (8 * g + r4) * 128 + q4 * 8;
  auto tfrag = [&](LAS unsigned char* tile, int s, int t) { LAS unsigned char* p = tile + s * 4096 + trow + ((t ^ fsw) << 5); return trfrag(p, p + 512); };
  bf16x8 xf[4]; unsigned ev;
  {
    const unsigned char* src = a.wq + (size_t)(xg * 512 + j * 64) * 2;
    const unsigned ve = (unsigned)((wave * 128 + prow) * 8192 + ce * 16), vo = (unsigned)((wave * 128 + 8 + prow) * 8192 + co * 16);
#pragma unroll
    for (int i = 0; i < ((EXP & 2) ? 0 : 8); ++i) { wpiece(src, ve + i * 16 * 8192, ldsb + T0_OFF + wave * 16384 + i * 2048); wpiece(src, vo + i * 16 * 8192, ldsb + T0_OFF + wave * 16384 + i * 2048 + 1024); }
  }
  {
    const unsigned xo = (unsigned)((n & 7) * 1024 + wave * 256 + g * 16), zero = 0;
    asm volatile("global_load_dword %4, %7, %8 sc1\n\tglobal_load_dwordx4 %0, %5, %6\n\tglobal_load_dwordx4 %1, %5, %6 offset:64\n\tglobal_load_dwordx4 %2, %5, %6 offset:128\n\tglobal_load_dwordx4 %3, %5, %6 offset:192\n\ts_waitcnt vmcnt(0)"
                 : "=&v"(xf[0]), "=&v"(xf[1]), "=&v"(xf[2]), "=&v"(xf[3]), "=&v"(ev) : "v"(xo), "s"(a.x), "v"(zero), "s"(a.ws) : "memory");
  }
  const unsigned tag = __builtin_amdgcn_readfirstlane(ev) + 1u;
  const int pl = (g * 8 + (n & 7)) * 16;
  auto proj = [&](LAS unsigned char* tile, LAS unsigned char* dst) {
    f32x4 c[4];
#pragma unroll
    for (int t = 0; t < 4; ++t) c[t] = f32x4{0.f, 0.f, 0.f, 0.f};
#pragma unroll
    for (int s = 0; s < 4; ++s)
#pragma unroll
      for (int t = 0; t < 4; ++t) c[t] = __builtin_amdgcn_mfma_f32_16x16x32_bf16(tfrag(tile, s, t), xf[s], c[t], 0, 0, 0);
    if (n < 8) {
#pragma unroll
      for (int t = 0; t < 4; ++t) *(LAS f32x4*)(dst + t * 512 + pl) = c[t];
    }
  };
  auto red4 = [&](LAS unsigned char* p) { return (*(const LAS f32x4*)p + *(const LAS f32x4*)(p + 2048)) + (*(const LAS f32x4*)(p + 4096) + *(const LAS f32x4*)(p + 6144)); };
  proj(lds + T0_OFF + wave * 16384, lds + QP_OFF + wave * 2048);
  wg_barrier();
  {
    const f32x4 c0 = red4(lds + QP_OFF + wave * 512 + pl);
    const unsigned val = n < 8 ? cvtpk(c0[0], c0[1]) : cvtpk(c0[2], c0[3]);
    put((gu64*)(a.ws + WS_QG) + (size_t)(xg * 8 + (n & 7)) * 256 + j * 32 + wave * 8 + g * 2 + (n >> 3), tag, val);
  }
  ST(1);
  const int head = (xg ^ HX) * 4 + (j >> 1);
  if (j < 7) {
    const size_t col = (size_t)(xg * 224 + j * 32) * 2;
    const unsigned char* se = (ce < 4 ? a.wg : a.wu) + (size_t)(wave * 128 + prow) * (FF * 2) + col + (ce & 3) * 16;
    const unsigned char* so = (co < 4 ? a.wg : a.wu) + (size_t)(wave * 128 + 8 + prow) * (FF * 2) + col + (co & 3) * 16;
#pragma unroll
    for (int i = 0; i < ((EXP & 1) ? 0 : 8); ++i) { wpiece64(se + (size_t)i * 16 * FF * 2, ldsb + T1_OFF + wave * 16384 + i * 2048); wpiece64(so + (size_t)i * 16 * FF * 2, ldsb + T1_OFF + wave * 16384 + i * 2048 + 1024); }
  }
  {
    const unsigned char* src = a.wo + ((size_t)(head * 128) * DM + (j & 1) * 256 + wave * 64) * 2;
    const unsigned ve = (unsigned)(prow * 1024 + ce * 16), vo = (unsigned)((8 + prow) * 1024 + co * 16);
#pragma unroll
    for (int i = 0; i < ((EXP & 8) ? 0 : 8); ++i) { wpiece(src, ve + i * 16 * 1024, ldsb + T0_OFF + wave * 16384 + i * 2048); wpiece(src, vo + i * 16 * 1024, ldsb + T0_OFF + wave * 16384 + i * 2048 + 1024); }
  }
  wait_vm<16>();
  if (j < 7) proj(lds + T1_OFF + wave * 16384, lds + GP_OFF + wave * 2048);
  wg_barrier();
  {
    const unsigned char* src = a.wd + ((size_t)(xg * 224) * DM + j * 64) * 2;
#pragma unroll
    for (int i = 0; i < ((EXP & 16) ? 0 : 7); ++i) { const int pc = wave * 7 + i; wpiece(src, (unsigned)((pc * 8 + prow) * 1024 + ((pc & 1) ? co : ce) * 16), ldsb + T1_OFF + pc * 1024); }
  }
  if (j < 7 && wave < 2) {
    const f32x4 cg = red4(lds + GP_OFF + wave * 512 + pl), cu = red4(lds + GP_OFF + (wave + 2) * 512 + pl);
    float hv[4];
#pragma unroll
    for (int k = 0; k < 4; ++k) hv[k] = rbf(sigmoidf_(rbf(cg[k]))) * rbf(cu[k]);
    const unsigned val = n < 8 ? cvtpk(hv[0], hv[1]) : cvtpk(hv[2], hv[3]);
    put((gu64*)(a.ws + WS_HG) + (size_t)(xg * 8 + (n & 7)) * 112 + j * 16 + wave * 8 + g * 2 + (n >> 3), tag, val);
  }
  ST(2);
  float gate;
  {
    const v4u xg8 = *(const GAS v4u*)(a.x + (size_t)j * 1024 + lane * 16), gw8 = *(const GAS v4u*)(a.gw + lane * 16);
    float s = 0.f;
#pragma unroll
    for (int k = 0; k < 4; ++k) {
      s += rbf(bf2f(xg8[k] & 0xffffu) * bf2f(gw8[k] & 0xffffu));
      s += rbf(bf2f(xg8[k] >> 16) * bf2f(gw8[k] >> 16));
    }
    s += dpp<0xB1>(s); s += dpp<0x4E>(s); s += dpp<0x141>(s); s += dpp<0x128>(s);
    s = xrow_sum(s);
    gate = rbf(sigmoidf_(rbf(s)));
  }
  bf16x8 wf[16];
  {
    v4u hy[7];
    const unsigned hg = WS_HG + (unsigned)(((xg * 8 + (n & 7)) * 112 + g * 4 + (n >> 3) * 2) * 8);
#pragma unroll 1
    for (unsigned it = 0; it < SPIN_ITERS; ++it) {
      bool ok = true;
#pragma unroll
      for (int s = 0; s < 7; ++s) { hy[s] = ld2g(wsr, hg + s * 128, 0); ok &= hy[s].y == tag && hy[s].w == tag; }
      if (__all(ok)) break;
      __builtin_amdgcn_s_sleep(8);
      asm volatile("" ::: "memory");
    }
    wait_vm<0>();
    wg_barrier();
    f32x4 c0 = {0.f, 0.f, 0.f, 0.f};
#pragma unroll
    for (int s = 0; s < 7; ++s) {
      const unsigned o0 = (unsigned)__builtin_amdgcn_mov_dpp((int)hy[s].x, 0x128, 0xf, 0xf, true), o1 = (unsigned)__builtin_amdgcn_mov_dpp((int)hy[s].z, 0x128, 0xf, 0xf, true);
      const v4u hf = n < 8 ? v4u{hy[s].x, hy[s].z, o0, o1} : v4u{o0, o1, hy[s].x, hy[s].z};
      c0 = __builtin_amdgcn_mfma_f32_16x16x32_bf16(tfrag(lds + T1_OFF, s, wave), __builtin_bit_cast(bf16x8, hf), c0, 0, 0, 0);
    }
    st2g(wsr, WS_DG + (unsigned)(((n & 7) * 32 + j * 4 + wave) * 1024 + (xg * 16 + g * 4 + (n >> 3) * 2) * 8), tag, n < 8 ? c0[0] : c0[2], n < 8 ? c0[1] : c0[3]);
#pragma unroll
    for (int s = 0; s < 4; ++s)
#pragma unroll
      for (int t = 0; t < 4; ++t) wf[s * 4 + t] = tfrag(lds + T0_OFF + wave * 16384, s, t);
  }
  ST(3);
  const int c16 = xg * 4 + wave;
  auto red8 = [&](float v) { v += dpp<0x128>(v); return xrow_sum(v); };
  float mb0, mb1;
  {
    v4u y;
#pragma unroll 1
    for (unsigned it = 0; it < SPIN_ITERS; ++it) {
      y = ld2g(wsr, WS_DG + (unsigned)((j * 32 + c16) * 1024 + lane * 16), 0);
      if (__all(y.y == tag && y.w == tag)) break;
      __builtin_amdgcn_s_sleep(8);
      asm volatile("" ::: "memory");
    }
    const float g1 = rbf(1.f - gate);
    mb0 = rbf(g1 * rbf(red8(__uint_as_float(y.x)))); mb1 = rbf(g1 * rbf(red8(__uint_as_float(y.z))));
  }
  if (PT & 4) {
#pragma unroll
    for (int k = 0; k < 4; ++k) { const v4u y = ld2g(wsr, WS_AG + (unsigned)((j * 32 + c16) * 4096 + lane * 16 + k * 1024), 0); asm volatile("" :: "v"(y)); }
  }
  ST(4);
  {
    const unsigned ag = WS_AG + (unsigned)(((n & 7) * 32 + (j & 1) * 16 + wave * 4) * 4096 + (head * 16 + g * 4 + (n >> 3) * 2) * 8);
    unsigned done = 0;
#pragma unroll 1
    for (unsigned it = 0; it < SPIN_ITERS * 8; ++it) {
      const v4u mw = *(volatile LAS v4u*)(lds + TAG_OFF);
      bf16x8 of[4];
#pragma unroll
      for (int s = 0; s < 4; ++s) of[s] = *(volatile LAS bf16x8*)(lds + O_OFF + (n & 7) * O_PITCH + s * 64 + g * 16);
      const unsigned fresh = __builtin_amdgcn_readfirstlane(mw.x | mw.y | mw.z | mw.w) & ~done;
      if (fresh) {
        ST(5);
        f32x4 c[4];
#pragma unroll
        for (int t = 0; t < 4; ++t) c[t] = f32x4{0.f, 0.f, 0.f, 0.f};
#pragma unroll
        for (int s = 0; s < 4; ++s)
#pragma unroll
          for (int t = 0; t < 4; ++t) c[t] = __builtin_amdgcn_mfma_f32_16x16x32_bf16(wf[s * 4 + t], of[s], c[t], 0, 0, 0);
        const unsigned dst = ag + (((fresh >> (n & 7)) & 1) ? 0u : OOB);
#pragma unroll
        for (int t = 0; t < 4; ++t) st2g(wsr, dst + t * 4096, tag, n < 8 ? c[t][0] : c[t][2], n < 8 ? c[t][1] : c[t][3]);
        done |= fresh;
        if (done == 255u) break;
      } else if (!done) __builtin_amdgcn_s_sleep(4); else if (CSLEEP) __builtin_amdgcn_s_sleep(CSLEEP);
    }
  }
  ST(6);
  {
    const unsigned ag = WS_AG + (unsigned)((j * 32 + c16) * 4096 + lane * 16);
    const v4u srd = {(unsigned)(size_t)a.ws, (unsigned)((size_t)a.ws >> 32) & 0xffffu, WS_END, 0x00020000u};
    v4u r[4]; u64 m[4];
#pragma unroll
    for (int k = 0; k < 4; ++k) { r[k] = v4u{0u, ~tag, 0u, ~tag}; m[k] = ~0ull; }
#pragma unroll 1
    for (unsigned it = 0; it < SPIN_ITERS; ++it) {
      poll4(r, m, srd, ag);
#pragma unroll
      for (int k = 0; k < 4; ++k) m[k] = waiting(r[k], tag);
      if (!(m[0] | m[1] | m[2] | m[3])) break;
    }
    const float A0 = rbf(red8((__uint_as_float(r[0].x) + __uint_as_float(r[1].x)) + (__uint_as_float(r[2].x) + __uint_as_float(r[3].x))));
    const float A1 = rbf(red8((__uint_as_float(r[0].z) + __uint_as_float(r[1].z)) + (__uint_as_float(r[2].z) + __uint_as_float(r[3].z))));
    const unsigned res = cvtpk(rbf(gate * A0) + mb0, rbf(gate * A1) + mb1);
    if (lane < 8) *(GAS unsigned*)(a.out + j * DM + c16 * 16 + lane * 2) = res;
  }
  ST(7);
  ST_FLUSH();
}

extern "C" void kernel_launch(void* const* d_in, const int* in_sizes, int n_in, void* d_out, int out_size, void* d_ws, size_t ws_size, hipStream_t stream) {
  Args a;
  a.x = (const unsigned char*)d_in[0]; a.K = (const unsigned char*)d_in[1]; a.V = (const unsigned char*)d_in[2];
  a.wq = (const unsigned char*)d_in[3]; a.wo = (const unsigned char*)d_in[4];
  a.wg = (const unsigned char*)d_in[6]; a.wu = (const unsigned char*)d_in[7]; a.wd = (const unsigned char*)d_in[8]; a.gw = (const unsigned char*)d_in[9];
  a.ws = (unsigned char*)d_ws; a.out = (u16*)d_out;
  (void)hipFuncSetAttribute((const void*)fused_kernel, hipFuncAttributeMaxDynamicSharedMemorySize, LDS_BYTES);
  fused_kernel<<<256, 512, LDS_BYTES, stream>>>(a);
}
```

```cpp
#include <hip/hip_runtime.h>
#include <hip/hip_bf16.h>
#include <cstdint>

#define GAS __attribute__((address_space(1)))
#define LAS __attribute__((address_space(3)))
typedef unsigned v4u __attribute__((ext_vector_type(4)));
typedef unsigned v2u __attribute__((ext_vector_type(2)));
typedef float f32x4 __attribute__((ext_vector_type(4)));
typedef float f32x2 __attribute__((ext_vector_type(2)));
typedef __bf16 bf16x8 __attribute__((ext_vector_type(8)));
typedef __bf16 bf16x2 __attribute__((ext_vector_type(2)));
typedef short s16x4 __attribute__((ext_vector_type(4)));
typedef unsigned short u16;
typedef unsigned long long u64;
typedef GAS u64 gu64;
typedef GAS unsigned gu32;
typedef __amdgpu_buffer_rsrc_t rsrc_t;
#define RLX_AGENT __ATOMIC_RELAXED, __HIP_MEMORY_SCOPE_AGENT

constexpr int DM = 512, HKV = 8, SK = 8192, FF = 1792;

__device__ __forceinline__ float bf2f(unsigned v) { return __builtin_bit_cast(float, v << 16); }
__device__ __forceinline__ unsigned cvtpk(float lo, float hi) { return __builtin_bit_cast(unsigned, __builtin_convertvector(f32x2{lo, hi}, bf16x2)); }
__device__ __forceinline__ float rbf(float x) { return __builtin_bit_cast(float, cvtpk(x, x) << 16); }
template <int CTRL> __device__ __forceinline__ float dpp(float x) {
  return __builtin_bit_cast(float, __builtin_amdgcn_mov_dpp(__builtin_bit_cast(int, x), CTRL, 0xf, 0xf, true));
}
__device__ __forceinline__ float xrow_max(float x) {
  auto s = __builtin_amdgcn_permlane16_swap(__float_as_uint(x), __float_as_uint(x), false, false);
  x = fmaxf(__uint_as_float(s[0]), __uint_as_float(s[1]));
  auto t = __builtin_amdgcn_permlane32_swap(__float_as_uint(x), __float_as_uint(x), false, false);
  return fmaxf(__uint_as_float(t[0]), __uint_as_float(t[1]));
}
__device__ __forceinline__ float xrow_sum(float x) {
  auto s = __builtin_amdgcn_permlane16_swap(__float_as_uint(x), __float_as_uint(x), false, false);
  x = __uint_as_float(s[0]) + __uint_as_float(s[1]);
  auto t = __builtin_amdgcn_permlane32_swap(__float_as_uint(x), __float_as_uint(x), false, false);
  return __uint_as_float(t[0]) + __uint_as_float(t[1]);
}
template <int N> __device__ __forceinline__ void wait_vm() { asm volatile("s_waitcnt vmcnt(%0)" ::"n"(N) : "memory"); }
__device__ __forceinline__ void wg_barrier() { asm volatile("s_waitcnt lgkmcnt(0)\n\ts_barrier" ::: "memory"); }
__device__ __forceinline__ u64 rt() { return __builtin_amdgcn_s_memrealtime(); }
__device__ __forceinline__ float sigmoidf_(float v) { return 1.f / (1.f + __expf(-v)); }

__device__ __forceinline__ void fill8(const unsigned char* base, const unsigned (&v)[4], unsigned dst) {
  unsigned keep;
  asm volatile(
      "s_mov_b32 %0, m0\n\t"
      "s_mov_b32 m0, %7\n\ts_nop 0\n\t"
      "global_load_lds_dwordx4 %1, %5 nt\n\t"
      "global_load_lds_dwordx4 %2, %5 offset:1024 nt\n\t"
      "global_load_lds_dwordx4 %3, %5 offset:2048 nt\n\t"
      "global_load_lds_dwordx4 %4, %5 offset:3072 nt\n\t"
      "s_bitset1_b32 m0, 12\n\ts_nop 0\n\t"
      "global_load_lds_dwordx4 %1, %6 nt\n\t"
      "global_load_lds_dwordx4 %2, %6 offset:1024 nt\n\t"
      "global_load_lds_dwordx4 %3, %6 offset:2048 nt\n\t"
      "global_load_lds_dwordx4 %4, %6 offset:3072 nt\n\t"
      "s_mov_b32 m0, %0"
      : "=&s"(keep)
      : "v"(v[0]), "v"(v[1]), "v"(v[2]), "v"(v[3]), "s"(base), "s"(base + 4096), "s"(dst)
      : "memory");
}
__device__ __forceinline__ void wpiece(const void* sbase, unsigned voff, unsigned dst) {
  unsigned keep;
  asm volatile("s_mov_b32 %0, m0\n\ts_mov_b32 m0, %3\n\ts_nop 0\n\tglobal_load_lds_dwordx4 %1, %2 nt\n\ts_mov_b32 m0, %0"
               : "=&s"(keep) : "v"(voff), "s"(sbase), "s"(dst) : "memory");
}
__device__ __forceinline__ void wslab(const unsigned char* src, unsigned row0, unsigned pitch, int n, unsigned dst, int lane) {
  unsigned vo = (row0 + (lane >> 3)) * pitch + (((lane & 7) ^ (2 * ((lane >> 4) & 1))) * 16);
#pragma unroll 1
  for (int i = 0; i < n; ++i) { wpiece(src, vo, dst); vo = (vo ^ 64) + 8 * pitch; dst += 1024; }
}
__device__ __forceinline__ bf16x8 trfrag(LAS unsigned char* p0, LAS unsigned char* p1) {
  const s16x4 a = __builtin_amdgcn_ds_read_tr16_b64_v4i16((LAS s16x4*)p0), b = __builtin_amdgcn_ds_read_tr16_b64_v4i16((LAS s16x4*)p1);
  return __builtin_bit_cast(bf16x8, __builtin_shufflevector(a, b, 0, 1, 2, 3, 4, 5, 6, 7));
}
__device__ __forceinline__ void put(gu64* p, unsigned tag, unsigned val) { __hip_atomic_store(p, ((u64)tag << 32) | val, RLX_AGENT); }
__device__ __forceinline__ v4u ld2g(rsrc_t r, unsigned voff, unsigned soff) { return __builtin_amdgcn_raw_buffer_load_b128(r, voff, soff, 16); }
__device__ __forceinline__ void st2g(rsrc_t r, unsigned voff, unsigned tag, float v0, float v1) {
  __builtin_amdgcn_raw_buffer_store_b128(v4u{__float_as_uint(v0), tag, __float_as_uint(v1), tag}, r, voff, 0, 16);
}
#define PLD(r, v, m, off) "s_mov_b64 exec, " m "\n\ts_cbranch_execz 2\n\tbuffer_load_dwordx4 " r ", " v ", %[srd], 0 offen offset:" off " sc1\n\t"
template <int STRIDE> __device__ __forceinline__ void poll3(v4u (&r)[3], const u64 (&m)[3], const v4u& srd, unsigned va) {
  asm volatile(PLD("%0", "%[va]", "%[m0]", "0") PLD("%1", "%[va]", "%[m1]", "%[s1]") PLD("%2", "%[va]", "%[m2]", "%[s2]")
               "s_mov_b64 exec, -1\n\ts_waitcnt vmcnt(0)"
               : "+v"(r[0]), "+v"(r[1]), "+v"(r[2])
               : [va] "v"(va), [srd] "s"(srd), [m0] "s"(m[0]), [m1] "s"(m[1]), [m2] "s"(m[2]), [s1] "n"(STRIDE), [s2] "n"(2 * STRIDE)
               : "memory");
}
__device__ __forceinline__ void poll4(v4u (&r)[4], const u64 (&m)[4], const v4u& srd, unsigned va) {
  asm volatile(PLD("%0", "%[va]", "%[m0]", "0") PLD("%1", "%[va]", "%[m1]", "1024") PLD("%2", "%[va]", "%[m2]", "2048") PLD("%3", "%[va]", "%[m3]", "3072")
               "s_mov_b64 exec, -1\n\ts_waitcnt vmcnt(0)"
               : "+v"(r[0]), "+v"(r[1]), "+v"(r[2]), "+v"(r[3])
               : [va] "v"(va), [srd] "s"(srd), [m0] "s"(m[0]), [m1] "s"(m[1]), [m2] "s"(m[2]), [m3] "s"(m[3])
               : "memory");
}
__device__ __forceinline__ u64 waiting(const v4u& r, unsigned tag) { return __builtin_amdgcn_uicmp(r.y, tag, 33) | __builtin_amdgcn_uicmp(r.w, tag, 33); }
constexpr unsigned OOB = 0x01000000u;
constexpr unsigned HOT_ITERS = 1u << 21;
#define SPIN for (unsigned it = 1; (it & 255u) || rt() - tstart < 50000000u; ++it)

constexpr int NPARK = 6;
constexpr unsigned QDELAY = 600, HDELAY = 1700, TQUIET = 3300;

constexpr int SLOT = 8192, RING = 4 * SLOT;
constexpr int MRG_REC = 132;
constexpr int P_OFF = 4 * RING;
constexpr int AL_OFF = P_OFF + 4 * 22 * 256;
constexpr int Q_OFF = AL_OFF + 4 * 22 * 16;
constexpr int ML_OFF = Q_OFF + 1024;
constexpr int TAG_OFF = ML_OFF + 128;
constexpr int QP_OFF = 4 * RING, GP_OFF = QP_OFF + 8192;
constexpr int O_OFF = GP_OFF + 8192, O_PITCH = 272;
static_assert(O_OFF + 8 * O_PITCH <= Q_OFF, "lds map");
constexpr int T0_OFF = 0, T1_OFF = 65536;
constexpr int LDS_BYTES = TAG_OFF + 16;
static_assert(LDS_BYTES <= 163840 && LDS_BYTES > 81920, "one workgroup per CU");

constexpr int PG_REC = 512;
constexpr unsigned WS_E = 0;
constexpr unsigned WS_QG = 4096;
constexpr unsigned WS_HG = WS_QG + 8 * 8 * 256 * 8;
constexpr unsigned WS_ML = WS_HG + 2 * 8 * 896 * 8;
constexpr unsigned WS_PG = WS_ML + 192 * 128;
constexpr unsigned WS_CG = 1024;
constexpr unsigned WS_PR = (WS_PG + 32 * 24 * PG_REC + 32767) / 32768 * 32768;
constexpr unsigned WS_AG = WS_PR + 32768;
constexpr unsigned WS_END = WS_AG + 8 * 32 * 4096;

static_assert(WS_END <= (2u << 20), "one 2 MB page");
__device__ __forceinline__ unsigned ag_item(int b, int c16, unsigned near) { const unsigned c = (c16 & 13) | ((((c16 >> 4) ^ near) & 1) << 1) | ((c16 & 2) << 3); return WS_AG + (unsigned)(b * 32 + c) * 4096; }
struct Args { const unsigned char *K, *V; unsigned char* ws; const unsigned char *x, *wq, *wo, *wg, *wu, *wd, *gw; u16* out; };

__global__ void __launch_bounds__(512, 2) fused_kernel(Args a) {
  asm volatile("" ::: "memory");
  __builtin_amdgcn_sched_barrier(0);
  extern __shared__ __attribute__((aligned(16))) unsigned char lds_[];
  LAS unsigned char* lds = (LAS unsigned char*)lds_;
  const u64 tstart = rt();
  const int tid = threadIdx.x, lane = tid & 63, wave = __builtin_amdgcn_readfirstlane(tid >> 6);
  const int bid = blockIdx.x, xg = bid & 7, idx = bid >> 3;
  const int n = lane & 15, g = lane >> 4, q4 = n & 3, r4 = n >> 2;
  if (idx < 8 && wave < 4) {
    wslab(a.wq + (size_t)(xg * 512 + idx * 64) * 2, wave * 128, 8192, 16, (unsigned)(size_t)lds + T0_OFF + wave * 16384, lane);
  }
  if (__builtin_expect(idx < 8 || wave >= 4, 0)) {
  if (idx >= 8) {
    if (wave > 4) __builtin_amdgcn_endpgm();
    __builtin_amdgcn_s_setprio(3);
    const rsrc_t wsr = __builtin_amdgcn_make_buffer_rsrc(a.ws, 0, (int)WS_END, 0x00020000);
    const unsigned tag = __builtin_amdgcn_readfirstlane(__hip_atomic_load((const gu32*)(a.ws + WS_E), RLX_AGENT)) + 1u;
    if (lane == 0) { *(LAS unsigned*)(lds + TAG_OFF) = tag; *(volatile LAS unsigned*)(lds + TAG_OFF + 4) = 0u; }
    wg_barrier();
    const unsigned qg = WS_QG + (unsigned)((((xg + idx) & 7) * 8 + (idx & 7)) * 2048 + lane * 16);
    v4u y0, y1;
    while (rt() - tstart < QDELAY) __builtin_amdgcn_s_sleep(8);
#pragma unroll 1
    SPIN {
      y0 = ld2g(wsr, qg, 0); y1 = ld2g(wsr, qg + 1024, 0);
      if (__all(y0.y == tag && y0.w == tag && y1.y == tag && y1.w == tag)) break;
      __builtin_amdgcn_s_sleep(24);
      asm volatile("" ::: "memory");
    }
    *(LAS v2u*)(lds + Q_OFF + lane * 8) = v2u{y0.x, y0.z};
    *(LAS v2u*)(lds + Q_OFF + 512 + lane * 8) = v2u{y1.x, y1.z};
    asm volatile("" ::: "memory");
    *(volatile LAS unsigned*)(lds + TAG_OFF + 4) = 1u;
    __builtin_amdgcn_endpgm();
  }
  if (wave >= 4) {
    const int gi = wave - 4;
    const rsrc_t wsr = __builtin_amdgcn_make_buffer_rsrc(a.ws, 0, (int)WS_END, 0x00020000);
    const unsigned tag = __builtin_amdgcn_readfirstlane(__hip_atomic_load((const gu32*)(a.ws + WS_E), RLX_AGENT)) + 1u;
    if (lane == 0) *(volatile LAS unsigned*)(lds + TAG_OFF + gi * 4) = 0u;
    asm volatile("" :: "s"(tag) : "memory");
    wg_barrier(); wg_barrier();
    __builtin_amdgcn_s_setprio(3);
    const unsigned grp = WS_PG + (unsigned)((xg * 4 + (idx >> 1)) * 24 + gi * 6) * PG_REC;
    const unsigned vml = WS_ML + (unsigned)((xg * 24 + gi * 6 + (lane < 6 ? lane : 0)) * 128 + (idx >> 1) * 16);
    const v4u srd = {(unsigned)(size_t)a.ws, (unsigned)((size_t)a.ws >> 32) & 0xffffu, WS_END, 0x00020000u};
    if (bid == 0 && gi == 1) {
      st2g(wsr, WS_PR + (unsigned)(lane >> 3) * 4096 + (lane & 7) * 16, tag, 0.f, 0.f);
      st2g(wsr, WS_PR + (unsigned)(lane >> 3) * 4096 + (lane & 7) * 16 + 2048, tag, 0.f, 0.f);
      wait_vm<0>();
      unsigned best[2] = {~0u, ~0u};
#pragma unroll 1
      for (int i = 0; i < 16; ++i) {
        u64 c0, c1; v4u y;
        asm volatile("s_memtime %0\n\ts_waitcnt lgkmcnt(0)\n\tbuffer_load_dwordx4 %2, %3, %4, 0 offen sc1\n\ts_waitcnt vmcnt(0)\n\ts_memtime %1\n\ts_waitcnt lgkmcnt(0)"
                     : "=&s"(c0), "=&s"(c1), "=&v"(y) : "v"(WS_PR + (unsigned)i * 2048), "s"(srd) : "memory");
        const unsigned d = (unsigned)(c1 - c0);
        if (i & 4) best[1] = min(best[1], d); else best[0] = min(best[0], d);
      }
      if (lane == 0) st2g(wsr, WS_CG, tag, best[1] < best[0] ? 1.f : 0.f, 0.f);
    }
    while (rt() - tstart < TQUIET) __builtin_amdgcn_s_sleep(32);
    if (bid == 0 && gi == 0) {
#pragma unroll 1
      SPIN {
        bool ok = true;
#pragma unroll
        for (int k = 0; k < 3; ++k) ok &= ld2g(wsr, WS_ML + (unsigned)(k * 64 + lane) * 128, 0).y == tag;
        if (__all(ok)) break;
        __builtin_amdgcn_s_sleep(8);
        asm volatile("" ::: "memory");
      }
      if (lane == 0) __hip_atomic_store((gu32*)(a.ws + WS_E), tag, RLX_AGENT);
    }
    float cw[6];
    {
      v4u y;
#pragma unroll 1
      SPIN {
        y = ld2g(wsr, vml, 0);
        if (__all(y.y == tag && y.w == tag)) break;
        __builtin_amdgcn_s_sleep(8);
        asm volatile("" ::: "memory");
      }
#pragma unroll
      for (int q = 0; q < 2; ++q) {
        float pm[3], pl[3];
#pragma unroll
        for (int k = 0; k < 3; ++k) { pm[k] = __uint_as_float(__builtin_amdgcn_readlane(y.x, 3 * q + k)); pl[k] = __uint_as_float(__builtin_amdgcn_readlane(y.z, 3 * q + k)); }
        const float MM = fmaxf(fmaxf(pm[0], pm[1]), pm[2]);
#pragma unroll
        for (int k = 0; k < 3; ++k) cw[3 * q + k] = __builtin_amdgcn_exp2f(pm[k] - MM);
        const float inv = 1.f / fmaf(cw[3 * q + 2], pl[2], fmaf(cw[3 * q + 1], pl[1], cw[3 * q] * pl[0]));
#pragma unroll
        for (int k = 0; k < 3; ++k) cw[3 * q + k] *= inv;
      }
    }
    v4u r[3]; u64 m[3];
    const unsigned vp2 = grp + (lane >> 5) * 3 * PG_REC + (lane & 31) * 16;
    const float c0 = lane < 32 ? cw[0] : cw[3], c1 = lane < 32 ? cw[1] : cw[4], c2 = lane < 32 ? cw[2] : cw[5];
#pragma unroll
    for (int s = 0; s < 3; ++s) { r[s] = v4u{0u, ~tag, 0u, ~tag}; m[s] = ~0ull; }
    unsigned done = 0;
#pragma unroll 1
    for (unsigned it = 0; it < HOT_ITERS; ++it) {
      poll3<PG_REC>(r, m, srd, vp2);
#pragma unroll
      for (int s = 0; s < 3; ++s) m[s] = waiting(r[s], tag);
      const u64 all = m[0] & m[1] & m[2], any = m[0] | m[1] | m[2];
      if ((all & 0x100000001ull) == 0x100000001ull) {
#pragma unroll
        for (int s = 0; s < 3; ++s) m[s] = 0x100000001ull;
        __builtin_amdgcn_s_sleep(4);
        continue;
      }
      const unsigned ready = ((unsigned)any ? 0u : 1u) | ((unsigned)(any >> 32) ? 0u : 2u);
      if (ready == done) continue;
      auto mix = [&](unsigned x0, unsigned x1, unsigned x2) {
        return cvtpk(fmaf(c2, bf2f(x2 & 0xffffu), fmaf(c1, bf2f(x1 & 0xffffu), c0 * bf2f(x0 & 0xffffu))), fmaf(c2, bf2f(x2 >> 16), fmaf(c1, bf2f(x1 >> 16), c0 * bf2f(x0 >> 16))));
      };
      *(volatile LAS v2u*)(lds + O_OFF + (gi * 2 + (lane >> 5)) * O_PITCH + (lane & 31) * 8) = v2u{mix(r[0].x, r[1].x, r[2].x), mix(r[0].z, r[1].z, r[2].z)};
      done = ready;
      *(volatile LAS unsigned*)(lds + TAG_OFF + gi * 4) = done << (2 * gi);
      if (done == 3u) break;
    }
    __builtin_amdgcn_endpgm();
  }
  const unsigned ldsb = (unsigned)(size_t)lds;
  const rsrc_t wsr = __builtin_amdgcn_make_buffer_rsrc(a.ws, 0, (int)WS_END, 0x00020000);
  const int j = idx;
  const int fsw = ((r4 >> 1) & 1) | ((g & 1) << 1);
  const int trow = (8 * g + r4) * 128 + q4 * 8;
  auto tfrag = [&](LAS unsigned char* tile, int s, int t) { LAS unsigned char* p = tile + s * 4096 + trow + ((t ^ fsw) << 5); return trfrag(p, p + 512); };
  bf16x8 xf[4]; unsigned ev;
  {
    const unsigned xo = (unsigned)((n & 7) * 1024 + wave * 256 + g * 16), zero = 0;
    asm volatile("global_load_dword %4, %7, %8 sc1\n\tglobal_load_dwordx4 %0, %5, %6\n\tglobal_load_dwordx4 %1, %5, %6 offset:64\n\tglobal_load_dwordx4 %2, %5, %6 offset:128\n\tglobal_load_dwordx4 %3, %5, %6 offset:192\n\ts_waitcnt vmcnt(0)"
                 : "=&v"(xf[0]), "=&v"(xf[1]), "=&v"(xf[2]), "=&v"(xf[3]), "=&v"(ev) : "v"(xo), "s"(a.x), "v"(zero), "s"(a.ws) : "memory");
  }
  const unsigned tag = __builtin_amdgcn_readfirstlane(ev) + 1u;
  const int pl = (g * 8 + (n & 7)) * 16;
  auto proj = [&](LAS unsigned char* tile, LAS unsigned char* dst) {
    f32x4 c[4];
#pragma unroll
    for (int t = 0; t < 4; ++t) c[t] = f32x4{0.f, 0.f, 0.f, 0.f};
#pragma unroll
    for (int s = 0; s < 4; ++s)
#pragma unroll
      for (int t = 0; t < 4; ++t) c[t] = __builtin_amdgcn_mfma_f32_16x16x32_bf16(tfrag(tile, s, t), xf[s], c[t], 0, 0, 0);
    if (n < 8) {
#pragma unroll
      for (int t = 0; t < 4; ++t) *(LAS f32x4*)(dst + t * 512 + pl) = c[t];
    }
  };
  const int rb = wave * 2 + (lane >> 5), rc = lane & 31;
  auto red4 = [&](LAS unsigned char* p0) {
    LAS unsigned char* p = p0 + (rc >> 3) * 512 + (((rc >> 1) & 3) * 8 + rb) * 16 + (rc & 1) * 8;
    return (*(const LAS f32x2*)p + *(const LAS f32x2*)(p + 2048)) + (*(const LAS f32x2*)(p + 4096) + *(const LAS f32x2*)(p + 6144));
  };
  proj(lds + T0_OFF + wave * 16384, lds + QP_OFF + wave * 2048);
  wg_barrier();
  {
    const f32x2 c0 = red4(lds + QP_OFF);
    put((gu64*)(a.ws + WS_QG) + (size_t)(xg * 8 + rb) * 256 + j * 32 + rc, tag, cvtpk(c0[0], c0[1]));
  }
  const int head = xg * 4 + (j >> 1);
  const int hp = xg * 7 + j, mt = hp & 1, cb = hp >> 1;
  if (j < 7) wslab((mt ? a.wu : a.wg) + cb * 128, wave * 128, FF * 2, 16, ldsb + T1_OFF + wave * 16384, lane);
  const unsigned col0 = (unsigned)((j & 1) * 256 + wave * 64) * 2;
  wslab(a.wo + col0, head * 128, DM * 2, 16, ldsb + T0_OFF + wave * 16384, lane);
  wait_vm<16>();
  if (j < 7) proj(lds + T1_OFF + wave * 16384, lds + GP_OFF + wave * 2048);
  wg_barrier();
  wslab(a.wd + col0, head * 56, DM * 2, 7, ldsb + T1_OFF + wave * 16384, lane);
  *(LAS v4u*)(lds + T1_OFF + wave * 16384 + 7 * 1024 + lane * 16) = v4u{0u, 0u, 0u, 0u};
  if (j < 7) {
    f32x2 c0 = red4(lds + GP_OFF);
    if (mt == 0) { c0[0] = sigmoidf_(rbf(c0[0])); c0[1] = sigmoidf_(rbf(c0[1])); }
    put((gu64*)(a.ws + WS_HG) + (size_t)(mt * 8 + rb) * 896 + cb * 32 + rc, tag, cvtpk(c0[0], c0[1]));
  }
  auto red8 = [&](float v) { v += dpp<0x128>(v); return xrow_sum(v); };
  float gate;
  {
    const unsigned part = (unsigned)(lane >> 3) * 128;
    float s = 0.f;
#pragma unroll 1
    for (int i = 0; i < 8; ++i) {
      const v4u xv = *(const GAS v4u*)(a.x + (n & 7) * 1024 + part + i * 16), gv = *(const GAS v4u*)(a.gw + part + i * 16);
#pragma unroll
      for (int k = 0; k < 4; ++k) {
        s += rbf(bf2f(xv[k] & 0xffffu) * bf2f(gv[k] & 0xffffu));
        s += rbf(bf2f(xv[k] >> 16) * bf2f(gv[k] >> 16));
      }
    }
    gate = rbf(sigmoidf_(rbf(red8(s))));
  }
  bf16x8 wf[16];
  f32x2 mb[4];
  {
    v4u gy[2], uy[2];
    const unsigned hg = WS_HG + (unsigned)(((n & 7) * 896 + head * 28 + g * 4 + (n >> 3) * 2) * 8), hg1 = g < 3 ? hg + 128 : hg;
    while (rt() - tstart < HDELAY) __builtin_amdgcn_s_sleep(16);
#pragma unroll 1
    SPIN {
      gy[0] = ld2g(wsr, hg, 0); uy[0] = ld2g(wsr, hg + 8 * 896 * 8, 0); gy[1] = ld2g(wsr, hg1, 0); uy[1] = ld2g(wsr, hg1 + 8 * 896 * 8, 0);
      if (!(waiting(gy[0], tag) | waiting(uy[0], tag) | waiting(gy[1], tag) | waiting(uy[1], tag))) break;
      __builtin_amdgcn_s_sleep(8);
      asm volatile("" ::: "memory");
    }
    wait_vm<0>();
    auto hmul = [&](unsigned sg, unsigned u) { return cvtpk(bf2f(sg & 0xffffu) * bf2f(u & 0xffffu), bf2f(sg >> 16) * bf2f(u >> 16)); };
    f32x4 c[4];
#pragma unroll
    for (int t = 0; t < 4; ++t) c[t] = f32x4{0.f, 0.f, 0.f, 0.f};
#pragma unroll
    for (int s = 0; s < 2; ++s) {
      const bool pad = s == 1 && g == 3;
      const unsigned h0 = pad ? 0u : hmul(gy[s].x, uy[s].x), h1 = pad ? 0u : hmul(gy[s].z, uy[s].z);
      const unsigned o0 = (unsigned)__builtin_amdgcn_mov_dpp((int)h0, 0x128, 0xf, 0xf, true), o1 = (unsigned)__builtin_amdgcn_mov_dpp((int)h1, 0x128, 0xf, 0xf, true);
      const v4u hf = n < 8 ? v4u{h0, h1, o0, o1} : v4u{o0, o1, h0, h1};
#pragma unroll
      for (int t = 0; t < 4; ++t) c[t] = __builtin_amdgcn_mfma_f32_16x16x32_bf16(tfrag(lds + T1_OFF + wave * 16384, s, t), __builtin_bit_cast(bf16x8, hf), c[t], 0, 0, 0);
    }
    const float g1 = rbf(1.f - gate);
#pragma unroll
    for (int t = 0; t < 4; ++t) mb[t] = f32x2{n < 8 ? c[t][0] : c[t][2], n < 8 ? c[t][1] : c[t][3]} * g1;
#pragma unroll
    for (int s = 0; s < 4; ++s)
#pragma unroll
      for (int t = 0; t < 4; ++t) wf[s * 4 + t] = tfrag(lds + T0_OFF + wave * 16384, s, t);
  }
  unsigned near = 0;
  while (rt() - tstart < TQUIET - 200) __builtin_amdgcn_s_sleep(32);
#pragma unroll 1
  SPIN {
    const v4u y = ld2g(wsr, WS_CG, 0);
    near = __builtin_amdgcn_readfirstlane(y.x) != 0u;
    if (!waiting(y, tag)) break;
    asm volatile("" ::: "memory");
    __builtin_amdgcn_s_sleep(8);
  }
  {
    const unsigned ag = ag_item(n & 7, (j & 1) * 16 + wave * 4, near) + (unsigned)((head * 16 + g * 4 + (n >> 3) * 2) * 8);
    unsigned done = 0;
#pragma unroll 1
    for (unsigned it = 0; it < 4 * HOT_ITERS; ++it) {
      const v4u mw = *(volatile LAS v4u*)(lds + TAG_OFF);
      bf16x8 of[4];
#pragma unroll
      for (int s = 0; s < 4; ++s) of[s] = *(volatile LAS bf16x8*)(lds + O_OFF + (n & 7) * O_PITCH + s * 64 + g * 16);
      const unsigned fresh = __builtin_amdgcn_readfirstlane(mw.x | mw.y | mw.z | mw.w) & ~done;
      if (fresh) {
        f32x4 c[4];
#pragma unroll
        for (int t = 0; t < 4; ++t) c[t] = f32x4{0.f, 0.f, 0.f, 0.f};
#pragma unroll
        for (int s = 0; s < 4; ++s)
#pragma unroll
          for (int t = 0; t < 4; ++t) c[t] = __builtin_amdgcn_mfma_f32_16x16x32_bf16(wf[s * 4 + t], of[s], c[t], 0, 0, 0);
        const unsigned dst = ag + (((fresh >> (n & 7)) & 1) ? 0u : OOB);
#pragma unroll
        for (int t = 0; t < 4; ++t) st2g(wsr, dst + (t & 1) * 4096 + (t >> 1) * 65536, tag, fmaf(gate, n < 8 ? c[t][0] : c[t][2], mb[t][0]), fmaf(gate, n < 8 ? c[t][1] : c[t][3], mb[t][1]));
        done |= fresh;
        if (done == 255u) break;
      } else if (!done) __builtin_amdgcn_s_sleep(4);
    }
  }
  {
    const int c16 = xg * 4 + wave;
    const unsigned ag = ag_item(j, c16, near) + lane * 16;
    const v4u srd = {(unsigned)(size_t)a.ws, (unsigned)((size_t)a.ws >> 32) & 0xffffu, WS_END, 0x00020000u};
    v4u r[4]; u64 m[4];
#pragma unroll
    for (int k = 0; k < 4; ++k) { r[k] = v4u{0u, ~tag, 0u, ~tag}; m[k] = ~0ull; }
#pragma unroll 1
    for (unsigned it = 0; it < HOT_ITERS; ++it) {
      poll4(r, m, srd, ag);
#pragma unroll
      for (int k = 0; k < 4; ++k) m[k] = waiting(r[k], tag);
      if (!(m[0] | m[1] | m[2] | m[3])) break;
    }
    const float A0 = (red8((__uint_as_float(r[0].x) + __uint_as_float(r[1].x)) + (__uint_as_float(r[2].x) + __uint_as_float(r[3].x))));
    const float A1 = (red8((__uint_as_float(r[0].z) + __uint_as_float(r[1].z)) + (__uint_as_float(r[2].z) + __uint_as_float(r[3].z))));
    const unsigned res = cvtpk(A0, A1);
    if (lane < 8) *(GAS unsigned*)(a.out + j * DM + c16 * 16 + lane * 2) = res;
  }
  __builtin_amdgcn_endpgm();
  }
  {
    const int kvh = (xg + idx) & 7, b = idx & 7, sp = (idx >> 3) - 1, pair = b * HKV + kvh, gw = wave * 3 + sp, NU = gw < 4 ? 22 : 21;
    const int rot = (gw >> 1) + 32 * b;
    const unsigned ring = (unsigned)(size_t)lds + wave * RING;
    const size_t toff = (size_t)(pair * 2 + (gw & 1)) << 20;
    const unsigned char* kb = a.K + toff;
    const unsigned char* vb = a.V + toff;
    unsigned voff[4];
#pragma unroll
    for (int i = 0; i < 4; ++i) voff[i] = (unsigned)(g * 256 + ((n ^ (g << 2) ^ (((i & 1) << 1) | (i >> 1))) * 16));
    auto src = [&](int f) { const bool v = f >= NU; return (v ? vb : kb) + (unsigned)(((v ? f - NU : f) * 6 + rot) & 127) * 8192; };
    fill8(src(0), voff, ring);
    fill8(src(1), voff, ring + SLOT);
    fill8(src(2), voff, ring + 2 * SLOT);
    const rsrc_t wsr = __builtin_amdgcn_make_buffer_rsrc(a.ws, 0, (int)WS_END, 0x00020000);
    const unsigned rr = ((r4 & 1) << 1) | (r4 >> 1), rg = ((g & 1) << 1) | (g >> 1);
    const unsigned kbase = ring + r4 * 1024 + q4 * 256 + (g ^ q4) * 64 + rr * 16;
    const unsigned vbase = ring + g * 1024 + r4 * 256 + (((q4 >> 1) ^ (r4 << 2) ^ rg) * 16) + (q4 & 1) * 8;
    auto readK = [&](unsigned so, bf16x8 (&kf)[8]) {
#pragma unroll
      for (int t = 0; t < 4; ++t) {
        LAS unsigned char* p = (LAS unsigned char*)(size_t)(kbase ^ (so + 16 * t));
        kf[t] = *(const LAS bf16x8*)p; kf[4 + t] = *(const LAS bf16x8*)(p + 4096);
      }
      asm volatile("s_waitcnt lgkmcnt(0)" : "+v"(kf[0]), "+v"(kf[1]), "+v"(kf[2]), "+v"(kf[3]), "+v"(kf[4]), "+v"(kf[5]), "+v"(kf[6]), "+v"(kf[7]) :: "memory");
    };
    wait_vm<16>();
    fill8(src(3), voff, ring + 3 * SLOT);
    LAS unsigned char* pbuf = lds + P_OFF + wave * (22 * 256) + (g * 4 + q4) * 16;
    LAS unsigned char* abuf = lds + AL_OFF + wave * (22 * 16);
    float m = -INFINITY, l = 0.f;
    unsigned resc = 0;
    unsigned tag;
    {
      bf16x8 kr[NPARK][8];
#pragma unroll
      for (int u = 0; u < NPARK; ++u) {
        wait_vm<16>();
        readK((u & 3) * SLOT, kr[u]);
        fill8(src(u + 4), voff, ring + (u & 3) * SLOT);
        if (u == 0) wg_barrier();
      }
#pragma unroll 1
      SPIN { if (__builtin_amdgcn_readfirstlane(*(volatile LAS unsigned*)(lds + TAG_OFF + 4))) break; __builtin_amdgcn_s_sleep(1); }
      asm volatile("" ::: "memory");
      bf16x8 qf[4];
#pragma unroll
      for (int t = 0; t < 4; ++t) qf[t] = *(const LAS bf16x8*)(lds + Q_OFF + q4 * 256 + g * 64 + t * 16);
      tag = __builtin_amdgcn_readfirstlane(*(const LAS unsigned*)(lds + TAG_OFF));
      constexpr float C2 = 0.088388347648318440f * 1.4426950408889634f;
      auto scores = [&](const bf16x8 (&kf)[8], int u) {
        f32x4 s0 = {0.f, 0.f, 0.f, 0.f}, s1 = {0.f, 0.f, 0.f, 0.f};
#pragma unroll
        for (int t = 0; t < 4; ++t) {
          s0 = __builtin_amdgcn_mfma_f32_16x16x32_bf16(kf[t], qf[t], s0, 0, 0, 0);
          s1 = __builtin_amdgcn_mfma_f32_16x16x32_bf16(kf[4 + t], qf[t], s1, 0, 0, 0);
        }
        float s[8];
#pragma unroll
        for (int j = 0; j < 4; ++j) { s[j] = s0[j] * C2; s[4 + j] = s1[j] * C2; }
        float pm = fmaxf(fmaxf(fmaxf(s[0], s[1]), fmaxf(s[2], s[3])), fmaxf(fmaxf(s[4], s[5]), fmaxf(s[6], s[7])));
        pm = xrow_max(pm);
        if (__any(pm > m)) {
          const float mn = fmaxf(m, pm);
          const float alpha = __builtin_amdgcn_exp2f(m - mn);
          m = mn; l *= alpha;
          if (lane < 4) *(LAS float*)(abuf + u * 16 + lane * 4) = alpha;
          resc |= 1u << u;
        }
        float p[8];
#pragma unroll
        for (int j = 0; j < 8; ++j) p[j] = __builtin_amdgcn_exp2f(s[j] - m);
        l += ((p[0] + p[1]) + (p[2] + p[3])) + ((p[4] + p[5]) + (p[6] + p[7]));
        if (r4 == 0) *(LAS v4u*)(pbuf + u * 256) = v4u{cvtpk(p[0], p[1]), cvtpk(p[2], p[3]), cvtpk(p[4], p[5]), cvtpk(p[6], p[7])};
      };
#pragma unroll
      for (int u = 0; u < NPARK; ++u) scores(kr[u], u);
#pragma unroll 1
      for (int u = NPARK; u < NU; ++u) {
        wait_vm<24>();
        bf16x8 kf[8];
        readK((u & 3) * SLOT, kf);
        fill8(src(u + 4), voff, ring + (u & 3) * SLOT);
        scores(kf, u);
      }
    }
    l = xrow_sum(l);
    if (lane < 4) *(LAS f32x2*)(lds + ML_OFF + (lane * 4 + wave) * 8) = f32x2{m, l};
    wg_barrier();
    float wt[4];
    const unsigned mine = WS_PG + (unsigned)((kvh * 4 + wave) * 24 + b * 3 + sp) * PG_REC;
    {
      auto stats = [&](int h, float (&w)[4], float& L) {
        const f32x4 x0 = *(const LAS f32x4*)(lds + ML_OFF + h * 32), x1 = *(const LAS f32x4*)(lds + ML_OFF + h * 32 + 16);
        const float M = fmaxf(fmaxf(x0[0], x0[2]), fmaxf(x1[0], x1[2]));
        w[0] = __builtin_amdgcn_exp2f(x0[0] - M); w[1] = __builtin_amdgcn_exp2f(x0[2] - M); w[2] = __builtin_amdgcn_exp2f(x1[0] - M); w[3] = __builtin_amdgcn_exp2f(x1[2] - M);
        L = fmaf(w[3], x1[3], fmaf(w[2], x1[1], fmaf(w[1], x0[3], w[0] * x0[1])));
        return M;
      };
      float L;
      if (wave == 3) { const float M = stats(q4, wt, L); if (lane < 8) st2g(wsr, WS_ML + (unsigned)((kvh * 24 + b * 3 + sp) * 128 + lane * 16), tag, M, L); }
      (void)stats(wave, wt, L);
    }
    f32x4 ox = {0.f, 0.f, 0.f, 0.f}, oy = ox;
    unsigned mw = ring + (4 + 32 * g + n) * 4, mr = (unsigned)(size_t)lds + (wave * MRG_REC + 4 + 2 * lane) * 4, po = mine + lane * 8;
    asm volatile("" : "+v"(mw), "+v"(mr), "+v"(po));
#pragma unroll 1
    for (int u = 0; u < NU; ++u) {
      const unsigned so = ((NU + u) & 3) * SLOT;
      const v4u pf = *(const LAS v4u*)(pbuf + u * 256);
      if ((resc >> u) & 1) { const f32x4 al = *(const LAS f32x4*)(abuf + u * 16); ox *= al; oy *= al; }
      bf16x8 pk[4];
#pragma unroll
      for (int k = 0; k < 4; ++k) pk[k] = __builtin_bit_cast(bf16x8, r4 == k ? pf : v4u{0u, 0u, 0u, 0u});
      unsigned va[8];
#pragma unroll
      for (int e = 0; e < 8; ++e) va[e] = vbase ^ (so + 32 * e);
      asm volatile("" : "+v"(pk[0]), "+v"(pk[1]), "+v"(pk[2]), "+v"(pk[3]), "+v"(va[0]), "+v"(va[1]), "+v"(va[2]), "+v"(va[3]), "+v"(va[4]), "+v"(va[5]), "+v"(va[6]), "+v"(va[7]));
      const int left = NU - 1 - u;
      wait_vm<24>();
      if (left < 3) { wait_vm<16>(); if (left < 2) { wait_vm<8>(); if (left < 1) wait_vm<0>(); } }
      bf16x8 vf[8];
#pragma unroll
      for (int e = 0; e < 8; ++e) { LAS unsigned char* p = (LAS unsigned char*)(size_t)va[e]; vf[e] = trfrag(p, p + 4096); }
      if (left >= 4) {
        asm volatile("s_waitcnt lgkmcnt(0)" : "+v"(vf[0]), "+v"(vf[1]), "+v"(vf[2]), "+v"(vf[3]), "+v"(vf[4]), "+v"(vf[5]), "+v"(vf[6]), "+v"(vf[7]) :: "memory");
        fill8(src(NU + u + 4), voff, ring + so);
      }
#pragma unroll
      for (int k = 0; k < 4; ++k) {
        ox = __builtin_amdgcn_mfma_f32_16x16x32_bf16(pk[k], vf[2 * k], ox, 0, 0, 0);
        oy = __builtin_amdgcn_mfma_f32_16x16x32_bf16(pk[k], vf[2 * k + 1], oy, 0, 0, 0);
      }
    }
    {
      LAS float* w = (LAS float*)(size_t)mw;
#pragma unroll
      for (int h = 0; h < 4; ++h) { w[h * MRG_REC] = ox[h]; w[h * MRG_REC + 16] = oy[h]; }
    }
    wg_barrier();
    f32x2 ov[4];
#pragma unroll
    for (int w = 0; w < 4; ++w) ov[w] = *(const LAS f32x2*)(size_t)(mr + w * RING);
    __builtin_amdgcn_raw_buffer_store_b64(v2u{cvtpk(fmaf(wt[3], ov[3][0], fmaf(wt[2], ov[2][0], fmaf(wt[1], ov[1][0], wt[0] * ov[0][0]))), fmaf(wt[3], ov[3][1], fmaf(wt[2], ov[2][1], fmaf(wt[1], ov[1][1], wt[0] * ov[0][1])))), tag}, wsr, po, 0, 16);
  }
}

extern "C" void kernel_launch(void* const* d_in, const int* in_sizes, int n_in, void* d_out, int out_size, void* d_ws, size_t ws_size, hipStream_t stream) {
  Args a;
  a.x = (const unsigned char*)d_in[0]; a.K = (const unsigned char*)d_in[1]; a.V = (const unsigned char*)d_in[2];
  a.wq = (const unsigned char*)d_in[3]; a.wo = (const unsigned char*)d_in[4];
  a.wg = (const unsigned char*)d_in[6]; a.wu = (const unsigned char*)d_in[7]; a.wd = (const unsigned char*)d_in[8]; a.gw = (const unsigned char*)d_in[9];
  a.ws = (unsigned char*)d_ws; a.out = (u16*)d_out;
  (void)hipFuncSetAttribute((const void*)fused_kernel, hipFuncAttributeMaxDynamicSharedMemorySize, LDS_BYTES);
  fused_kernel<<<256, 512, LDS_BYTES, stream>>>(a);
}
```
